```python
import torch

def _softmax(x, axis=-1):
    return torch.softmax(x, dim=axis)
def _gelu(x):
    return torch.nn.functional.gelu(x, approximate="tanh")
def _silu(x):
    return torch.nn.functional.silu(x)
def _sigmoid(x):
    return torch.sigmoid(x)
def _layernorm(x, axis=-1, eps=1e-5):
    m = x.mean(dim=axis, keepdim=True)
    v = x.var(dim=axis, keepdim=True, unbiased=False)
    return (x - m) / torch.sqrt(v + eps)
def _scatter_add(tgt, idx, src, axis=0):
    return tgt.clone().index_add_(axis, idx, src)
def _segment_sum(src, seg, n):
    out = torch.zeros((n,) + src.shape[1:], dtype=src.dtype)
    return out.index_add_(0, seg, src)
def _pad(x, pw):
    flat = tuple(v for pair in reversed(pw) for v in pair)
    return torch.nn.functional.pad(x, flat)

kernel_name = 'concat_heads__linear_kernel_hedgehog__mha_block__prompt'

def setup_inputs():

    _w = (lambda a: (lambda t: t.to(torch.bfloat16) if t.is_floating_point() else t)(torch.as_tensor(a)))
    def _A_setup():
        B, S, D, H = 1, 2048, 640, 32
        Dh = D // H
        return {
            "x": _w((lambda *s: torch.randn(*s, dtype=torch.bfloat16) * 0.4)(B, S, D)),
            "wqkv": _w((lambda *s: torch.randn(*s, dtype=torch.bfloat16) * 0.4)(D, 3 * D)),
            "w_phi": _w((lambda *s: torch.randn(*s, dtype=torch.bfloat16) * 0.4)(H, Dh, Dh) * (Dh ** -0.5)),
            "wo": _w((lambda *s: torch.randn(*s, dtype=torch.bfloat16) * 0.05)(D, D)),
        }
    _A_in = _A_setup()

    def _B_setup():
        B, S, D, H = 1, 2048, 640, 32
        return {
            "x": _w((lambda *s: torch.randn(*s, dtype=torch.bfloat16) * 0.2)(B, S, D)),
            "wqkv": _w((lambda *s: torch.randn(*s, dtype=torch.bfloat16) * 0.2)(D, 3 * D)),
            "wo": _w((lambda *s: torch.randn(*s, dtype=torch.bfloat16) * 0.05)(D, D)),
        }
    _B_in = _B_setup()

    return {
        "A_x": _A_in["x"],
        "A_wqkv": _A_in["wqkv"],
        "A_w_phi": _A_in["w_phi"],
        "A_wo": _A_in["wo"],
        "B_x": _B_in["x"],
        "B_wqkv": _B_in["wqkv"],
        "B_wo": _B_in["wo"],
    }

def reference(A_x, A_wqkv, A_w_phi, A_wo, B_x, B_wqkv, B_wo):
    def _A_ref(x, wqkv, w_phi, wo):
        B, S, D = x.shape
        H = 32; Dh = D // H
        qkv = (lambda x, *d: x.permute(*d))((x @ wqkv).reshape(B, S, 3, H, Dh), 2, 0, 3, 1, 4)
        q, k, v = qkv[0], qkv[1], qkv[2]
        pq = torch.einsum("bhsd,hdf->bhsf", q, w_phi)
        pk = torch.einsum("bhsd,hdf->bhsf", k, w_phi)
        phi_q = torch.concatenate([_softmax(pq, axis=-1), _softmax(-pq, axis=-1)], axis=-1)
        phi_k = torch.concatenate([_softmax(pk, axis=-1), _softmax(-pk, axis=-1)], axis=-1)
        kv = torch.einsum("bhsf,bhsd->bhfd", phi_k, v)
        z  = torch.einsum("bhsf->bhf", phi_k)
        num = torch.einsum("bhqf,bhfd->bhqd", phi_q, kv)
        den = torch.einsum("bhqf,bhf->bhq", phi_q, z)[..., None] + 1e-6
        o = num / den
        return (lambda x, *d: x.permute(*d))(o, 0, 2, 1, 3).reshape(B, S, D) @ wo

    def _B_ref(x, wqkv, wo):
        B, S, D = x.shape
        H = 32
        Dh = D // H
        qkv = (lambda x, *d: x.permute(*d))((x @ wqkv).reshape(B, S, 3, H, Dh), 2, 0, 3, 1, 4)
        q, k, v = qkv[0], qkv[1], qkv[2]
        scores = torch.einsum("bhqd,bhkd->bhqk", q, k) * (Dh ** -0.5)
        p = _softmax(scores, axis=-1)
        o = torch.einsum("bhqk,bhkd->bhqd", p, v)
        return (lambda x, *d: x.permute(*d))(o, 0, 2, 1, 3).reshape(B, S, D) @ wo

    a = _A_ref(A_x, A_wqkv, A_w_phi, A_wo)
    b = _B_ref(B_x, B_wqkv, B_wo)
    return torch.concatenate([a, b], axis=-1)
```

```cpp
#include <hip/hip_runtime.h>
#include <hip/hip_bf16.h>
typedef __hip_bfloat16 bf16;
static __device__ __forceinline__ float b2f(unsigned short u){ return __uint_as_float(((unsigned)u)<<16);}
static __device__ __forceinline__ unsigned short f2b(float f){ unsigned u=__float_as_uint(f); u += 0x7fffu + ((u>>16)&1u); return (unsigned short)(u>>16);}
typedef unsigned short u16;
__global__ void gemm(const u16* A, const u16* B, float* C, u16* Cb, int M, int N, int K, int ldc) {
  int n = blockIdx.x*64 + threadIdx.x, m = blockIdx.y;
  float s=0; for (int k=0;k<K;k++) s += b2f(A[m*K+k])*b2f(B[k*N+n]);
  if (C) C[m*ldc+n] = b2f(f2b(s)); else Cb[m*ldc+n] = f2b(s);
}
__global__ __launch_bounds__(256) void phi(const float* qkv, const u16* wphi, float* ph  ) {
  int idx = blockIdx.x*blockDim.x+threadIdx.x;
  int w = idx/(2048*32), s=(idx/32)%2048, h=idx%32;
  float p[20]; float mx=-1e30f, mn=1e30f;
  #pragma unroll
  for (int f=0;f<20;f++){ float a=0; for(int d=0;d<20;d++) a+= qkv[s*1920+w*640+h*20+d]*b2f(wphi[h*400+d*20+f]); p[f]=a; mx=fmaxf(mx,a); mn=fminf(mn,a);}
  float s1=0,s2=0;
  #pragma unroll
  for (int f=0;f<20;f++){ s1+=__expf(p[f]-mx); s2+=__expf(mn-p[f]);}
  #pragma unroll
  for (int f=0;f<20;f++){ ph[(long)idx*40+f]=__expf(p[f]-mx)/s1; ph[(long)idx*40+20+f]=__expf(mn-p[f])/s2;}
}
__global__ void kvred(const float* qkv, const float* ph, float* kv  ) {
  int h=blockIdx.x; int t=threadIdx.x; if (t>=840) return; int f=t/21,d=t%21; float a=0;
  for(int s=0;s<2048;s++){ float v = d<20? qkv[s*1920+1280+h*20+d]:1.f; a+= ph[((long)(2048*32)+s*32+h)*40+f]*v;} kv[h*840+t]=a;
}
__global__ void applyA(const float* ph, const float* kv, u16* o) {
  int idx = blockIdx.x*blockDim.x+threadIdx.x; int s=idx/640, c=idx%640, h=c/20,d=c%20; float n=0,dn=0;
  for(int f=0;f<40;f++){ float p=ph[((long)s*32+h)*40+f]; n+=p*kv[h*840+f*21+d]; dn+=p*kv[h*840+f*21+20];}
  o[idx]=f2b(n/(dn+1e-6f));
}
__global__ void attn(const float* qkv, u16* o) {
  int idx = blockIdx.x*blockDim.x+threadIdx.x; int h=idx/2048, s=idx%2048;
  float q[20], acc[20];
  #pragma unroll
  for(int d=0;d<20;d++){q[d]=qkv[s*1920+h*20+d]*0.22360679775f; acc[d]=0;}
  float m=-1e30f,l=0;
  for(int k=0;k<2048;k++){ float a=0;
    #pragma unroll
    for(int d=0;d<20;d++) a+=q[d]*qkv[k*1920+640+h*20+d];
    float mn=fmaxf(m,a); float c=__expf(m-mn), p=__expf(a-mn); l=l*c+p;
    #pragma unroll
    for(int d=0;d<20;d++) acc[d]=acc[d]*c+p*qkv[k*1920+1280+h*20+d];
    m=mn; }
  #pragma unroll
  for(int d=0;d<20;d++) o[s*640+h*20+d]=f2b(acc[d]/l);
}
extern "C" void kernel_launch(void* const* d_in, const int* in_sizes, int n_in, void* d_out, int out_size, void* d_ws, size_t ws_size, hipStream_t stream) {
  const u16* Ax=(const u16*)d_in[0]; const u16* Aw=(const u16*)d_in[1]; const u16* Ap=(const u16*)d_in[2]; const u16* Ao=(const u16*)d_in[3];
  const u16* Bx=(const u16*)d_in[4]; const u16* Bw=(const u16*)d_in[5]; const u16* Bo=(const u16*)d_in[6];
  float* qa=(float*)d_ws; float* qb=qa+2048*1920; float* ph=qb+2048*1920; float* kv=ph+2*2048*32*40; u16* oa=(u16*)(kv+32*840); u16* ob=oa+2048*640;
  u16* out=(u16*)d_out;
  gemm<<<dim3(30,2048),64,0,stream>>>(Ax,Aw,qa,nullptr,2048,1920,640,1920);
  gemm<<<dim3(30,2048),64,0,stream>>>(Bx,Bw,qb,nullptr,2048,1920,640,1920);
  phi<<<2*2048*32/256,256,0,stream>>>(qa,Ap,ph);
  kvred<<<32,896,0,stream>>>(qa,ph,kv);
  applyA<<<2048*640/256,256,0,stream>>>(ph,kv,oa);
  attn<<<2048*32/64,64,0,stream>>>(qb,ob);
  gemm<<<dim3(10,2048),64,0,stream>>>(oa,Ao,nullptr,out,2048,640,640,1280);
  gemm<<<dim3(10,2048),64,0,stream>>>(ob,Bo,nullptr,out+640,2048,640,640,1280);
}
```

```cpp
#include <hip/hip_runtime.h>
#include <hip/hip_bf16.h>
#include <stdint.h>

typedef short bf16x8 __attribute__((ext_vector_type(8)));
typedef short bf16x4 __attribute__((ext_vector_type(4)));
typedef float f32x16 __attribute__((ext_vector_type(16)));
typedef float f32x4 __attribute__((ext_vector_type(4)));
typedef float f32x2 __attribute__((ext_vector_type(2)));
typedef __bf16 bfx2 __attribute__((ext_vector_type(2)));
typedef unsigned u32x4 __attribute__((ext_vector_type(4)));
typedef unsigned u32x2 __attribute__((ext_vector_type(2)));
typedef unsigned short u16;
typedef unsigned long long u64;
#define DI __device__ __forceinline__
#define LAS __attribute__((address_space(3)))
#define GAS __attribute__((address_space(1)))
#define RLX_AGENT __ATOMIC_RELAXED, __HIP_MEMORY_SCOPE_AGENT
#define RLX_WG __ATOMIC_RELAXED, __HIP_MEMORY_SCOPE_WORKGROUP
#define MFMA16(a, b, c) __builtin_amdgcn_mfma_f32_32x32x16_bf16(__builtin_bit_cast(__attribute__((ext_vector_type(8))) __bf16, a), __builtin_bit_cast(__attribute__((ext_vector_type(8))) __bf16, b), c, 0, 0, 0)
#define MFMA8(a, b, c) __builtin_amdgcn_mfma_f32_32x32x8bf16_1k(__builtin_bit_cast(bf16x4, a), __builtin_bit_cast(bf16x4, b), c, 0, 0, 0)
#define L_MIN 8.27e-25f
constexpr int DRAIN = 3;
constexpr int CODE_BYTES = 38912;
#define SC1 16

constexpr unsigned SEC = 2621440;
constexpr unsigned V_G = 2176, V_T = 4 * V_G, V_C = 64 * V_T;
constexpr unsigned WS_BR = 2 * SEC + 5 * V_C;
constexpr unsigned OOB = 0x80000000u;
constexpr unsigned WS_O = 2 * WS_BR;
constexpr unsigned O_B = 2097152;
constexpr unsigned WS_KVP = WS_O + 2 * SEC;
constexpr unsigned WS_FLG = WS_KVP + 256 * 4096;
constexpr unsigned FS = 128, F_B = 1, F_A = 257, F_KV = 513, F_O = 769;

DI unsigned rfl(unsigned x) { return __builtin_amdgcn_readfirstlane(x); }
DI unsigned pk2(float lo, float hi) { f32x2 v = {lo, hi}; return __builtin_bit_cast(unsigned, __builtin_convertvector(v, bfx2)); }
DI u32x4 pk8(const f32x16& a, int o, float s) { return (u32x4){pk2(a[o] * s, a[o + 1] * s), pk2(a[o + 2] * s, a[o + 3] * s), pk2(a[o + 4] * s, a[o + 5] * s), pk2(a[o + 6] * s, a[o + 7] * s)}; }
DI u32x4 pk8(const f32x16& a, int o) { return (u32x4){pk2(a[o], a[o + 1]), pk2(a[o + 2], a[o + 3]), pk2(a[o + 4], a[o + 5]), pk2(a[o + 6], a[o + 7])}; }
DI u32x4 ld16(__amdgpu_buffer_rsrc_t r, unsigned off, unsigned so = 0) { return __builtin_amdgcn_raw_buffer_load_b128(r, off, so, SC1); }
DI u32x2 ld8(__amdgpu_buffer_rsrc_t r, unsigned off, unsigned so = 0) { return __builtin_amdgcn_raw_buffer_load_b64(r, off, so, SC1); }
DI GAS u64* flagp(char* ws, unsigned i) { return (GAS u64*)(ws + WS_FLG + i * FS); }
DI void wait_flags(char* ws, unsigned idx, u64 tag) {
    GAS u64* p = flagp(ws, idx);
#pragma unroll 1
    for (unsigned n = 0; n < (1u << 20); n++) { if (__all(__hip_atomic_load(p, RLX_AGENT) == tag)) break; __builtin_amdgcn_s_sleep(1); }
    __builtin_amdgcn_fence(__ATOMIC_ACQUIRE, "wavefront");
}
template <int SLEEP> DI void wait_lds(LAS unsigned* p, unsigned v) {
#pragma unroll 1
    for (unsigned n = 0; n < (1u << 24); n++) { if (rfl(__hip_atomic_load(p, RLX_WG)) >= v) break; if (SLEEP) __builtin_amdgcn_s_sleep(SLEEP * 3); }
    __builtin_amdgcn_fence(__ATOMIC_ACQUIRE, "wavefront");
}
DI u64 call_tag(LAS unsigned* ctl) {
    const u32x2 e = *(LAS u32x2*)ctl;
    u64 z = (((u64)rfl(e[1]) << 32) | rfl(e[0])) + 0x9E3779B97F4A7C15ull;
    z = (z ^ (z >> 30)) * 0xBF58476D1CE4E5B9ull; z = (z ^ (z >> 27)) * 0x94D049BB133111EBull;
    return z ^ (z >> 31);
}

struct Loader { unsigned xo[4], wo[4]; };
DI void stage_next(const Loader& L, const char* xs, const char* wsrc, unsigned dst) {
    unsigned sv;
    asm volatile("s_waitcnt vmcnt(8)\n\ts_barrier\n\ts_mov_b32 %0, m0\n\ts_mov_b32 m0, %11\n\ts_nop 0\n\t"
                 "global_load_lds_dwordx4 %1, %9\n\ts_add_u32 m0, m0, 0x1000\n\ts_nop 0\n\tglobal_load_lds_dwordx4 %2, %9\n\ts_add_u32 m0, m0, 0x1000\n\ts_nop 0\n\t"
                 "global_load_lds_dwordx4 %3, %9\n\ts_add_u32 m0, m0, 0x1000\n\ts_nop 0\n\tglobal_load_lds_dwordx4 %4, %9\n\ts_add_u32 m0, m0, 0x1000\n\ts_nop 0\n\t"
                 "global_load_lds_dwordx4 %5, %10\n\ts_add_u32 m0, m0, 0x1000\n\ts_nop 0\n\tglobal_load_lds_dwordx4 %6, %10\n\ts_add_u32 m0, m0, 0x1000\n\ts_nop 0\n\t"
                 "global_load_lds_dwordx4 %7, %10\n\ts_add_u32 m0, m0, 0x1000\n\ts_nop 0\n\tglobal_load_lds_dwordx4 %8, %10\n\ts_mov_b32 m0, %0"
                 : "=&s"(sv) : "v"(L.xo[0]), "v"(L.xo[1]), "v"(L.xo[2]), "v"(L.xo[3]), "v"(L.wo[0]), "v"(L.wo[1]), "v"(L.wo[2]), "v"(L.wo[3]), "s"(xs), "s"(wsrc), "s"(dst) : "memory", "scc");
}
struct Comp { unsigned xa[2], wa[4], so[4], on; };
typedef float f32x32 __attribute__((ext_vector_type(32)));
#define GEMM_OPERANDS(EXTRA) : "=&{v[64:95]}"(a0), "=&{v[96:127]}"(a1), [xa0] "+v"(C.xa[0]), [xa1] "+v"(C.xa[1]), [wa0] "+v"(C.wa[0]), [wa1] "+v"(C.wa[1]), [wa2] "+v"(C.wa[2]), [wa3] "+v"(C.wa[3]), \
      [slot] "+s"(slot), [cnt] "+s"(cnt), [t0] "=&s"(t0) : [so0] "v"(C.so[0]), [so1] "v"(C.so[1]), [so2] "v"(C.so[2]), [so3] "v"(C.so[3]), [srd] "s"(srd), [sc] "s"(sc), [dr] "s"(dr) EXTRA : "memory", "scc", "v0", "v1", "v2", "v3", "v4", "v5", "v6", "v7", "v8", "v9", "v10", "v11", "v12", "v13", "v14", "v15", "v16", "v17", "v18", "v19", "v20", "v21", "v22", "v23", "v24", "v25", "v26", "v27", "v28", "v29", "v30", "v31", "v32", "v33", "v34", "v35", "v36", "v37", "v38", "v39", "v40", "v41", "v42", "v43", "v44", "v45", "v46", "v47", "v48", "v49", "v50", "v51", "v52", "v53", "v54", "v55", "v56", "v57", "v58", "v59", "v60", "v61", "v62", "v63", "v128", "v129", "v130", "v131", "v132", "v133", "v134", "v135", "v136", "v137", "v138", "v139", "v140", "v141", "v142", "v143", "v144", "v145", "v146", "v147", "v148", "v149", "v150", "v151", "v152", "v153", "v154", "v155", "v156", "v157", "v158", "v159", "v160", "v161", "v162", "v163", "v164", "v165", "v166", "v167", "v168", "v169", "v170", "v171", "v172", "v173", "v174", "v175", "v176", "v177", "v178", "v179", "v180", "v181", "v182", "v183", "v184", "v185", "v186", "v187", "v188", "v189", "v190", "v191", "v192", "v193", "v194", "v195", "v196", "v197", "v198", "v199"
DI void gemm_wx(Comp& C, u32x4 srd, float sc, unsigned dr, f32x32& a0, f32x32& a1) {
    unsigned cnt = 10, slot = 0, t0;
    asm volatile(
        "s_barrier\n\t"
        "ds_read_b128 v[128:131], %[xa0] offset:0\n\t"
        "ds_read_b64_tr_b16 v[144:145], %[wa0] offset:16384\n\t"
        "ds_read_b64_tr_b16 v[146:147], %[wa0] offset:17408\n\t"
        "ds_read_b64_tr_b16 v[148:149], %[wa1] offset:16384\n\t"
        "ds_read_b64_tr_b16 v[150:151], %[wa1] offset:17408\n\t"
        "ds_read_b64_tr_b16 v[152:153], %[wa2] offset:16384\n\t"
        "ds_read_b64_tr_b16 v[154:155], %[wa2] offset:17408\n\t"
        "ds_read_b64_tr_b16 v[156:157], %[wa3] offset:16384\n\t"
        "ds_read_b64_tr_b16 v[158:159], %[wa3] offset:17408\n\t"
        "ds_read_b128 v[132:135], %[xa0] offset:2048\n\t"
        "ds_read_b128 v[136:139], %[xa0] offset:4096\n\t"
        "ds_read_b128 v[140:143], %[xa0] offset:6144\n\t"
        "s_waitcnt lgkmcnt(3)\n\t"
        "v_mfma_f32_16x16x32_bf16 v[0:3], v[144:147], v[128:131], 0\n\t"
        "ds_read_b128 v[160:163], %[xa1] offset:0\n\t"
        "v_mfma_f32_16x16x32_bf16 v[4:7], v[148:151], v[128:131], 0\n\t"
        "ds_read_b64_tr_b16 v[176:177], %[wa0] offset:24576\n\t"
        "s_add_u32 %[t0], %[slot], 0x8000\n\t"
        "v_mfma_f32_16x16x32_bf16 v[8:11], v[152:155], v[128:131], 0\n\t"
        "ds_read_b64_tr_b16 v[178:179], %[wa0] offset:25600\n\t"
        "s_and_b32 %[t0], %[t0], 0x1ffff\n\t"
        "v_mfma_f32_16x16x32_bf16 v[12:15], v[156:159], v[128:131], 0\n\t"
        "ds_read_b64_tr_b16 v[180:181], %[wa1] offset:24576\n\t"
        "s_xor_b32 %[slot], %[slot], %[t0]\n\t"
        "s_waitcnt lgkmcnt(6)\n\t"
        "v_mfma_f32_16x16x32_bf16 v[16:19], v[144:147], v[132:135], 0\n\t"
        "ds_read_b64_tr_b16 v[182:183], %[wa1] offset:25600\n\t"
        "v_mfma_f32_16x16x32_bf16 v[20:23], v[148:151], v[132:135], 0\n\t"
        "ds_read_b64_tr_b16 v[184:185], %[wa2] offset:24576\n\t"
        "v_mfma_f32_16x16x32_bf16 v[24:27], v[152:155], v[132:135], 0\n\t"
        "ds_read_b64_tr_b16 v[186:187], %[wa2] offset:25600\n\t"
        "v_mfma_f32_16x16x32_bf16 v[28:31], v[156:159], v[132:135], 0\n\t"
        "ds_read_b64_tr_b16 v[188:189], %[wa3] offset:24576\n\t"
        "s_waitcnt lgkmcnt(9)\n\t"
        "v_mfma_f32_16x16x32_bf16 v[32:35], v[144:147], v[136:139], 0\n\t"
        "ds_read_b64_tr_b16 v[190:191], %[wa3] offset:25600\n\t"
        "v_mfma_f32_16x16x32_bf16 v[36:39], v[148:151], v[136:139], 0\n\t"
        "ds_read_b128 v[164:167], %[xa1] offset:2048\n\t"
        "v_mfma_f32_16x16x32_bf16 v[40:43], v[152:155], v[136:139], 0\n\t"
        "ds_read_b128 v[168:171], %[xa1] offset:4096\n\t"
        "v_mfma_f32_16x16x32_bf16 v[44:47], v[156:159], v[136:139], 0\n\t"
        "ds_read_b128 v[172:175], %[xa1] offset:6144\n\t"
        "s_waitcnt lgkmcnt(12)\n\t"
        "v_mfma_f32_16x16x32_bf16 v[48:51], v[144:147], v[140:143], 0\n\t"
        "v_xor_b32 %[xa0], %[slot], %[xa0]\n\t"
        "v_xor_b32 %[xa1], %[slot], %[xa1]\n\t"
        "v_mfma_f32_16x16x32_bf16 v[52:55], v[148:151], v[140:143], 0\n\t"
        "v_xor_b32 %[wa0], %[slot], %[wa0]\n\t"
        "v_xor_b32 %[wa1], %[slot], %[wa1]\n\t"
        "v_mfma_f32_16x16x32_bf16 v[56:59], v[152:155], v[140:143], 0\n\t"
        "v_xor_b32 %[wa2], %[slot], %[wa2]\n\t"
        "v_xor_b32 %[wa3], %[slot], %[wa3]\n\t"
        "v_mfma_f32_16x16x32_bf16 v[60:63], v[156:159], v[140:143], 0\n\t"
        "s_mov_b32 %[slot], %[t0]\n\t"
        "s_branch 2f\n\t"
        "1:\n\t"
        "s_waitcnt lgkmcnt(3)\n\t"
        "v_mfma_f32_16x16x32_bf16 v[0:3], v[144:147], v[128:131], v[0:3]\n\t"
        "ds_read_b128 v[160:163], %[xa1] offset:0\n\t"
        "v_mfma_f32_16x16x32_bf16 v[4:7], v[148:151], v[128:131], v[4:7]\n\t"
        "ds_read_b64_tr_b16 v[176:177], %[wa0] offset:24576\n\t"
        "s_add_u32 %[t0], %[slot], 0x8000\n\t"
        "v_mfma_f32_16x16x32_bf16 v[8:11], v[152:155], v[128:131], v[8:11]\n\t"
        "ds_read_b64_tr_b16 v[178:179], %[wa0] offset:25600\n\t"
        "s_and_b32 %[t0], %[t0], 0x1ffff\n\t"
        "v_mfma_f32_16x16x32_bf16 v[12:15], v[156:159], v[128:131], v[12:15]\n\t"
        "ds_read_b64_tr_b16 v[180:181], %[wa1] offset:24576\n\t"
        "s_xor_b32 %[slot], %[slot], %[t0]\n\t"
        "s_waitcnt lgkmcnt(6)\n\t"
        "v_mfma_f32_16x16x32_bf16 v[16:19], v[144:147], v[132:135], v[16:19]\n\t"
        "ds_read_b64_tr_b16 v[182:183], %[wa1] offset:25600\n\t"
        "v_mfma_f32_16x16x32_bf16 v[20:23], v[148:151], v[132:135], v[20:23]\n\t"
        "ds_read_b64_tr_b16 v[184:185], %[wa2] offset:24576\n\t"
        "v_mfma_f32_16x16x32_bf16 v[24:27], v[152:155], v[132:135], v[24:27]\n\t"
        "ds_read_b64_tr_b16 v[186:187], %[wa2] offset:25600\n\t"
        "v_mfma_f32_16x16x32_bf16 v[28:31], v[156:159], v[132:135], v[28:31]\n\t"
        "ds_read_b64_tr_b16 v[188:189], %[wa3] offset:24576\n\t"
        "s_waitcnt lgkmcnt(9)\n\t"
        "v_mfma_f32_16x16x32_bf16 v[32:35], v[144:147], v[136:139], v[32:35]\n\t"
        "ds_read_b64_tr_b16 v[190:191], %[wa3] offset:25600\n\t"
        "v_mfma_f32_16x16x32_bf16 v[36:39], v[148:151], v[136:139], v[36:39]\n\t"
        "ds_read_b128 v[164:167], %[xa1] offset:2048\n\t"
        "v_mfma_f32_16x16x32_bf16 v[40:43], v[152:155], v[136:139], v[40:43]\n\t"
        "ds_read_b128 v[168:171], %[xa1] offset:4096\n\t"
        "v_mfma_f32_16x16x32_bf16 v[44:47], v[156:159], v[136:139], v[44:47]\n\t"
        "ds_read_b128 v[172:175], %[xa1] offset:6144\n\t"
        "s_waitcnt lgkmcnt(12)\n\t"
        "v_mfma_f32_16x16x32_bf16 v[48:51], v[144:147], v[140:143], v[48:51]\n\t"
        "v_xor_b32 %[xa0], %[slot], %[xa0]\n\t"
        "v_xor_b32 %[xa1], %[slot], %[xa1]\n\t"
        "v_mfma_f32_16x16x32_bf16 v[52:55], v[148:151], v[140:143], v[52:55]\n\t"
        "v_xor_b32 %[wa0], %[slot], %[wa0]\n\t"
        "v_xor_b32 %[wa1], %[slot], %[wa1]\n\t"
        "v_mfma_f32_16x16x32_bf16 v[56:59], v[152:155], v[140:143], v[56:59]\n\t"
        "v_xor_b32 %[wa2], %[slot], %[wa2]\n\t"
        "v_xor_b32 %[wa3], %[slot], %[wa3]\n\t"
        "v_mfma_f32_16x16x32_bf16 v[60:63], v[156:159], v[140:143], v[60:63]\n\t"
        "s_mov_b32 %[slot], %[t0]\n\t"
        "2:\n\t"
        "s_waitcnt lgkmcnt(0)\n\t"
        "s_barrier\n\t"
        "v_mfma_f32_16x16x32_bf16 v[0:3], v[176:179], v[160:163], v[0:3]\n\t"
        "ds_read_b128 v[128:131], %[xa0] offset:0\n\t"
        "v_mfma_f32_16x16x32_bf16 v[4:7], v[180:183], v[160:163], v[4:7]\n\t"
        "ds_read_b64_tr_b16 v[144:145], %[wa0] offset:16384\n\t"
        "v_mfma_f32_16x16x32_bf16 v[8:11], v[184:187], v[160:163], v[8:11]\n\t"
        "ds_read_b64_tr_b16 v[146:147], %[wa0] offset:17408\n\t"
        "v_mfma_f32_16x16x32_bf16 v[12:15], v[188:191], v[160:163], v[12:15]\n\t"
        "ds_read_b64_tr_b16 v[148:149], %[wa1] offset:16384\n\t"
        "v_mfma_f32_16x16x32_bf16 v[16:19], v[176:179], v[164:167], v[16:19]\n\t"
        "ds_read_b64_tr_b16 v[150:151], %[wa1] offset:17408\n\t"
        "v_mfma_f32_16x16x32_bf16 v[20:23], v[180:183], v[164:167], v[20:23]\n\t"
        "ds_read_b64_tr_b16 v[152:153], %[wa2] offset:16384\n\t"
        "v_mfma_f32_16x16x32_bf16 v[24:27], v[184:187], v[164:167], v[24:27]\n\t"
        "ds_read_b64_tr_b16 v[154:155], %[wa2] offset:17408\n\t"
        "v_mfma_f32_16x16x32_bf16 v[28:31], v[188:191], v[164:167], v[28:31]\n\t"
        "ds_read_b64_tr_b16 v[156:157], %[wa3] offset:16384\n\t"
        "v_mfma_f32_16x16x32_bf16 v[32:35], v[176:179], v[168:171], v[32:35]\n\t"
        "ds_read_b64_tr_b16 v[158:159], %[wa3] offset:17408\n\t"
        "v_mfma_f32_16x16x32_bf16 v[36:39], v[180:183], v[168:171], v[36:39]\n\t"
        "ds_read_b128 v[132:135], %[xa0] offset:2048\n\t"
        "v_mfma_f32_16x16x32_bf16 v[40:43], v[184:187], v[168:171], v[40:43]\n\t"
        "ds_read_b128 v[136:139], %[xa0] offset:4096\n\t"
        "v_mfma_f32_16x16x32_bf16 v[44:47], v[188:191], v[168:171], v[44:47]\n\t"
        "ds_read_b128 v[140:143], %[xa0] offset:6144\n\t"
        "v_mfma_f32_16x16x32_bf16 v[48:51], v[176:179], v[172:175], v[48:51]\n\t"
        "v_mfma_f32_16x16x32_bf16 v[52:55], v[180:183], v[172:175], v[52:55]\n\t"
        "v_mfma_f32_16x16x32_bf16 v[56:59], v[184:187], v[172:175], v[56:59]\n\t"
        "v_mfma_f32_16x16x32_bf16 v[60:63], v[188:191], v[172:175], v[60:63]\n\t"
        "s_sub_u32 %[cnt], %[cnt], 1\n\t"
        "s_cmp_lg_u32 %[cnt], 0\n\t"
        "s_cbranch_scc1 1b\n\t"
        "s_waitcnt lgkmcnt(3)\n\t"
        "v_mfma_f32_16x16x32_bf16 v[64:67], v[144:147], v[128:131], 0\n\t"
        "ds_read_b128 v[160:163], %[xa1] offset:0\n\t"
        "v_mfma_f32_16x16x32_bf16 v[68:71], v[148:151], v[128:131], 0\n\t"
        "ds_read_b64_tr_b16 v[176:177], %[wa0] offset:24576\n\t"
        "s_add_u32 %[t0], %[slot], 0x8000\n\t"
        "v_mfma_f32_16x16x32_bf16 v[72:75], v[152:155], v[128:131], 0\n\t"
        "ds_read_b64_tr_b16 v[178:179], %[wa0] offset:25600\n\t"
        "s_and_b32 %[t0], %[t0], 0x1ffff\n\t"
        "v_mfma_f32_16x16x32_bf16 v[76:79], v[156:159], v[128:131], 0\n\t"
        "ds_read_b64_tr_b16 v[180:181], %[wa1] offset:24576\n\t"
        "s_xor_b32 %[slot], %[slot], %[t0]\n\t"
        "s_waitcnt lgkmcnt(6)\n\t"
        "v_mfma_f32_16x16x32_bf16 v[80:83], v[144:147], v[132:135], 0\n\t"
        "ds_read_b64_tr_b16 v[182:183], %[wa1] offset:25600\n\t"
        "v_mul_f32 v0, %[sc], v0\n\t"
        "v_mul_f32 v1, %[sc], v1\n\t"
        "v_mfma_f32_16x16x32_bf16 v[84:87], v[148:151], v[132:135], 0\n\t"
        "ds_read_b64_tr_b16 v[184:185], %[wa2] offset:24576\n\t"
        "v_mul_f32 v2, %[sc], v2\n\t"
        "v_mul_f32 v3, %[sc], v3\n\t"
        "v_mfma_f32_16x16x32_bf16 v[88:91], v[152:155], v[132:135], 0\n\t"
        "ds_read_b64_tr_b16 v[186:187], %[wa2] offset:25600\n\t"
        "v_mul_f32 v16, %[sc], v16\n\t"
        "v_mul_f32 v17, %[sc], v17\n\t"
        "v_mfma_f32_16x16x32_bf16 v[92:95], v[156:159], v[132:135], 0\n\t"
        "ds_read_b64_tr_b16 v[188:189], %[wa3] offset:24576\n\t"
        "v_mul_f32 v18, %[sc], v18\n\t"
        "v_mul_f32 v19, %[sc], v19\n\t"
        "s_waitcnt lgkmcnt(9)\n\t"
        "v_mfma_f32_16x16x32_bf16 v[96:99], v[144:147], v[136:139], 0\n\t"
        "ds_read_b64_tr_b16 v[190:191], %[wa3] offset:25600\n\t"
        "v_cvt_pk_bf16_f32 v192, v0, v1\n\t"
        "v_cvt_pk_bf16_f32 v194, v16, v17\n\t"
        "v_mfma_f32_16x16x32_bf16 v[100:103], v[148:151], v[136:139], 0\n\t"
        "ds_read_b128 v[164:167], %[xa1] offset:2048\n\t"
        "v_cvt_pk_bf16_f32 v193, v2, v3\n\t"
        "v_cvt_pk_bf16_f32 v195, v18, v19\n\t"
        "v_mfma_f32_16x16x32_bf16 v[104:107], v[152:155], v[136:139], 0\n\t"
        "ds_read_b128 v[168:171], %[xa1] offset:4096\n\t"
        "v_permlane16_swap_b32 v192, v194\n\t"
        "v_permlane16_swap_b32 v193, v195\n\t"
        "v_mfma_f32_16x16x32_bf16 v[108:111], v[156:159], v[136:139], 0\n\t"
        "ds_read_b128 v[172:175], %[xa1] offset:6144\n\t"
        "buffer_store_dwordx4 v[192:195], %[so0], %[srd], 0 offen offset:0 sc1\n\t"
        "v_mul_f32 v4, %[sc], v4\n\t"
        "s_waitcnt lgkmcnt(12)\n\t"
        "v_mfma_f32_16x16x32_bf16 v[112:115], v[144:147], v[140:143], 0\n\t"
        "v_xor_b32 %[xa0], %[slot], %[xa0]\n\t"
        "v_xor_b32 %[xa1], %[slot], %[xa1]\n\t"
        "v_mfma_f32_16x16x32_bf16 v[116:119], v[148:151], v[140:143], 0\n\t"
        "v_xor_b32 %[wa0], %[slot], %[wa0]\n\t"
        "v_xor_b32 %[wa1], %[slot], %[wa1]\n\t"
        "v_mfma_f32_16x16x32_bf16 v[120:123], v[152:155], v[140:143], 0\n\t"
        "v_xor_b32 %[wa2], %[slot], %[wa2]\n\t"
        "v_xor_b32 %[wa3], %[slot], %[wa3]\n\t"
        "v_mfma_f32_16x16x32_bf16 v[124:127], v[156:159], v[140:143], 0\n\t"
        "s_mov_b32 %[slot], %[t0]\n\t"
        "s_waitcnt lgkmcnt(0)\n\t"
        "s_barrier\n\t"
        "v_mfma_f32_16x16x32_bf16 v[64:67], v[176:179], v[160:163], v[64:67]\n\t"
        "ds_read_b128 v[128:131], %[xa0] offset:0\n\t"
        "v_mul_f32 v5, %[sc], v5\n\t"
        "v_mul_f32 v6, %[sc], v6\n\t"
        "v_mfma_f32_16x16x32_bf16 v[68:71], v[180:183], v[160:163], v[68:71]\n\t"
        "ds_read_b64_tr_b16 v[144:145], %[wa0] offset:16384\n\t"
        "v_mul_f32 v7, %[sc], v7\n\t"
        "v_mul_f32 v20, %[sc], v20\n\t"
        "v_mfma_f32_16x16x32_bf16 v[72:75], v[184:187], v[160:163], v[72:75]\n\t"
        "ds_read_b64_tr_b16 v[146:147], %[wa0] offset:17408\n\t"
        "v_mul_f32 v21, %[sc], v21\n\t"
        "v_mul_f32 v22, %[sc], v22\n\t"
        "v_mfma_f32_16x16x32_bf16 v[76:79], v[188:191], v[160:163], v[76:79]\n\t"
        "ds_read_b64_tr_b16 v[148:149], %[wa1] offset:16384\n\t"
        "v_mul_f32 v23, %[sc], v23\n\t"
        "v_cvt_pk_bf16_f32 v196, v4, v5\n\t"
        "v_mfma_f32_16x16x32_bf16 v[80:83], v[176:179], v[164:167], v[80:83]\n\t"
        "ds_read_b64_tr_b16 v[150:151], %[wa1] offset:17408\n\t"
        "v_cvt_pk_bf16_f32 v198, v20, v21\n\t"
        "v_cvt_pk_bf16_f32 v197, v6, v7\n\t"
        "v_mfma_f32_16x16x32_bf16 v[84:87], v[180:183], v[164:167], v[84:87]\n\t"
        "ds_read_b64_tr_b16 v[152:153], %[wa2] offset:16384\n\t"
        "v_cvt_pk_bf16_f32 v199, v22, v23\n\t"
        "v_permlane16_swap_b32 v196, v198\n\t"
        "v_mfma_f32_16x16x32_bf16 v[88:91], v[184:187], v[164:167], v[88:91]\n\t"
        "ds_read_b64_tr_b16 v[154:155], %[wa2] offset:17408\n\t"
        "v_permlane16_swap_b32 v197, v199\n\t"
        "buffer_store_dwordx4 v[196:199], %[so1], %[srd], 0 offen offset:0 sc1\n\t"
        "v_mfma_f32_16x16x32_bf16 v[92:95], v[188:191], v[164:167], v[92:95]\n\t"
        "ds_read_b64_tr_b16 v[156:157], %[wa3] offset:16384\n\t"
        "v_mul_f32 v8, %[sc], v8\n\t"
        "v_mul_f32 v9, %[sc], v9\n\t"
        "v_mfma_f32_16x16x32_bf16 v[96:99], v[176:179], v[168:171], v[96:99]\n\t"
        "ds_read_b64_tr_b16 v[158:159], %[wa3] offset:17408\n\t"
        "v_mul_f32 v10, %[sc], v10\n\t"
        "v_mul_f32 v11, %[sc], v11\n\t"
        "v_mfma_f32_16x16x32_bf16 v[100:103], v[180:183], v[168:171], v[100:103]\n\t"
        "ds_read_b128 v[132:135], %[xa0] offset:2048\n\t"
        "v_mul_f32 v24, %[sc], v24\n\t"
        "v_mul_f32 v25, %[sc], v25\n\t"
        "v_mfma_f32_16x16x32_bf16 v[104:107], v[184:187], v[168:171], v[104:107]\n\t"
        "ds_read_b128 v[136:139], %[xa0] offset:4096\n\t"
        "v_mul_f32 v26, %[sc], v26\n\t"
        "v_mul_f32 v27, %[sc], v27\n\t"
        "v_mfma_f32_16x16x32_bf16 v[108:111], v[188:191], v[168:171], v[108:111]\n\t"
        "ds_read_b128 v[140:143], %[xa0] offset:6144\n\t"
        "v_cvt_pk_bf16_f32 v192, v8, v9\n\t"
        "v_cvt_pk_bf16_f32 v194, v24, v25\n\t"
        "v_mfma_f32_16x16x32_bf16 v[112:115], v[176:179], v[172:175], v[112:115]\n\t"
        "v_cvt_pk_bf16_f32 v193, v10, v11\n\t"
        "v_cvt_pk_bf16_f32 v195, v26, v27\n\t"
        "v_mfma_f32_16x16x32_bf16 v[116:119], v[180:183], v[172:175], v[116:119]\n\t"
        "v_permlane16_swap_b32 v192, v194\n\t"
        "v_permlane16_swap_b32 v193, v195\n\t"
        "v_mfma_f32_16x16x32_bf16 v[120:123], v[184:187], v[172:175], v[120:123]\n\t"
        "buffer_store_dwordx4 v[192:195], %[so2], %[srd], 0 offen offset:0 sc1\n\t"
        "v_mul_f32 v12, %[sc], v12\n\t"
        "v_mfma_f32_16x16x32_bf16 v[124:127], v[188:191], v[172:175], v[124:127]\n\t"
        "v_mul_f32 v13, %[sc], v13\n\t"
        "v_mul_f32 v14, %[sc], v14\n\t"
        "s_waitcnt lgkmcnt(3)\n\t"
        "v_mfma_f32_16x16x32_bf16 v[64:67], v[144:147], v[128:131], v[64:67]\n\t"
        "ds_read_b128 v[160:163], %[xa1] offset:0\n\t"
        "v_mul_f32 v15, %[sc], v15\n\t"
        "v_mul_f32 v28, %[sc], v28\n\t"
        "v_mfma_f32_16x16x32_bf16 v[68:71], v[148:151], v[128:131], v[68:71]\n\t"
        "ds_read_b64_tr_b16 v[176:177], %[wa0] offset:24576\n\t"
        "s_add_u32 %[t0], %[slot], 0x8000\n\t"
        "v_mul_f32 v29, %[sc], v29\n\t"
        "v_mul_f32 v30, %[sc], v30\n\t"
        "v_mfma_f32_16x16x32_bf16 v[72:75], v[152:155], v[128:131], v[72:75]\n\t"
        "ds_read_b64_tr_b16 v[178:179], %[wa0] offset:25600\n\t"
        "s_and_b32 %[t0], %[t0], 0x1ffff\n\t"
        "v_mul_f32 v31, %[sc], v31\n\t"
        "v_cvt_pk_bf16_f32 v196, v12, v13\n\t"
        "v_mfma_f32_16x16x32_bf16 v[76:79], v[156:159], v[128:131], v[76:79]\n\t"
        "ds_read_b64_tr_b16 v[180:181], %[wa1] offset:24576\n\t"
        "s_xor_b32 %[slot], %[slot], %[t0]\n\t"
        "v_cvt_pk_bf16_f32 v198, v28, v29\n\t"
        "v_cvt_pk_bf16_f32 v197, v14, v15\n\t"
        "s_waitcnt lgkmcnt(6)\n\t"
        "v_mfma_f32_16x16x32_bf16 v[80:83], v[144:147], v[132:135], v[80:83]\n\t"
        "ds_read_b64_tr_b16 v[182:183], %[wa1] offset:25600\n\t"
        "v_cvt_pk_bf16_f32 v199, v30, v31\n\t"
        "v_permlane16_swap_b32 v196, v198\n\t"
        "v_mfma_f32_16x16x32_bf16 v[84:87], v[148:151], v[132:135], v[84:87]\n\t"
        "ds_read_b64_tr_b16 v[184:185], %[wa2] offset:24576\n\t"
        "v_permlane16_swap_b32 v197, v199\n\t"
        "buffer_store_dwordx4 v[196:199], %[so3], %[srd], 0 offen offset:0 sc1\n\t"
        "v_mfma_f32_16x16x32_bf16 v[88:91], v[152:155], v[132:135], v[88:91]\n\t"
        "ds_read_b64_tr_b16 v[186:187], %[wa2] offset:25600\n\t"
        "v_mul_f32 v32, %[sc], v32\n\t"
        "v_mul_f32 v33, %[sc], v33\n\t"
        "v_mfma_f32_16x16x32_bf16 v[92:95], v[156:159], v[132:135], v[92:95]\n\t"
        "ds_read_b64_tr_b16 v[188:189], %[wa3] offset:24576\n\t"
        "v_mul_f32 v34, %[sc], v34\n\t"
        "v_mul_f32 v35, %[sc], v35\n\t"
        "s_waitcnt lgkmcnt(9)\n\t"
        "v_mfma_f32_16x16x32_bf16 v[96:99], v[144:147], v[136:139], v[96:99]\n\t"
        "ds_read_b64_tr_b16 v[190:191], %[wa3] offset:25600\n\t"
        "v_mul_f32 v48, %[sc], v48\n\t"
        "v_mul_f32 v49, %[sc], v49\n\t"
        "v_mfma_f32_16x16x32_bf16 v[100:103], v[148:151], v[136:139], v[100:103]\n\t"
        "ds_read_b128 v[164:167], %[xa1] offset:2048\n\t"
        "v_mul_f32 v50, %[sc], v50\n\t"
        "v_mul_f32 v51, %[sc], v51\n\t"
        "v_mfma_f32_16x16x32_bf16 v[104:107], v[152:155], v[136:139], v[104:107]\n\t"
        "ds_read_b128 v[168:171], %[xa1] offset:4096\n\t"
        "v_cvt_pk_bf16_f32 v192, v32, v33\n\t"
        "v_cvt_pk_bf16_f32 v194, v48, v49\n\t"
        "v_mfma_f32_16x16x32_bf16 v[108:111], v[156:159], v[136:139], v[108:111]\n\t"
        "ds_read_b128 v[172:175], %[xa1] offset:6144\n\t"
        "v_cvt_pk_bf16_f32 v193, v34, v35\n\t"
        "v_cvt_pk_bf16_f32 v195, v50, v51\n\t"
        "s_waitcnt lgkmcnt(12)\n\t"
        "v_mfma_f32_16x16x32_bf16 v[112:115], v[144:147], v[140:143], v[112:115]\n\t"
        "v_xor_b32 %[xa0], %[slot], %[xa0]\n\t"
        "v_xor_b32 %[xa1], %[slot], %[xa1]\n\t"
        "v_mfma_f32_16x16x32_bf16 v[116:119], v[148:151], v[140:143], v[116:119]\n\t"
        "v_xor_b32 %[wa0], %[slot], %[wa0]\n\t"
        "v_xor_b32 %[wa1], %[slot], %[wa1]\n\t"
        "v_mfma_f32_16x16x32_bf16 v[120:123], v[152:155], v[140:143], v[120:123]\n\t"
        "v_xor_b32 %[wa2], %[slot], %[wa2]\n\t"
        "v_xor_b32 %[wa3], %[slot], %[wa3]\n\t"
        "v_mfma_f32_16x16x32_bf16 v[124:127], v[156:159], v[140:143], v[124:127]\n\t"
        "s_mov_b32 %[slot], %[t0]\n\t"
        "s_waitcnt lgkmcnt(0)\n\t"
        "s_barrier\n\t"
        "v_mfma_f32_16x16x32_bf16 v[64:67], v[176:179], v[160:163], v[64:67]\n\t"
        "ds_read_b128 v[128:131], %[xa0] offset:0\n\t"
        "v_permlane16_swap_b32 v192, v194\n\t"
        "v_permlane16_swap_b32 v193, v195\n\t"
        "v_mfma_f32_16x16x32_bf16 v[68:71], v[180:183], v[160:163], v[68:71]\n\t"
        "ds_read_b64_tr_b16 v[144:145], %[wa0] offset:16384\n\t"
        "buffer_store_dwordx4 v[192:195], %[so0], %[srd], 0 offen offset:512 sc1\n\t"
        "v_mul_f32 v36, %[sc], v36\n\t"
        "v_mfma_f32_16x16x32_bf16 v[72:75], v[184:187], v[160:163], v[72:75]\n\t"
        "ds_read_b64_tr_b16 v[146:147], %[wa0] offset:17408\n\t"
        "v_mul_f32 v37, %[sc], v37\n\t"
        "v_mul_f32 v38, %[sc], v38\n\t"
        "v_mfma_f32_16x16x32_bf16 v[76:79], v[188:191], v[160:163], v[76:79]\n\t"
        "ds_read_b64_tr_b16 v[148:149], %[wa1] offset:16384\n\t"
        "v_mul_f32 v39, %[sc], v39\n\t"
        "v_mul_f32 v52, %[sc], v52\n\t"
        "v_mfma_f32_16x16x32_bf16 v[80:83], v[176:179], v[164:167], v[80:83]\n\t"
        "ds_read_b64_tr_b16 v[150:151], %[wa1] offset:17408\n\t"
        "v_mul_f32 v53, %[sc], v53\n\t"
        "v_mul_f32 v54, %[sc], v54\n\t"
        "v_mfma_f32_16x16x32_bf16 v[84:87], v[180:183], v[164:167], v[84:87]\n\t"
        "ds_read_b64_tr_b16 v[152:153], %[wa2] offset:16384\n\t"
        "v_mul_f32 v55, %[sc], v55\n\t"
        "v_cvt_pk_bf16_f32 v196, v36, v37\n\t"
        "v_mfma_f32_16x16x32_bf16 v[88:91], v[184:187], v[164:167], v[88:91]\n\t"
        "ds_read_b64_tr_b16 v[154:155], %[wa2] offset:17408\n\t"
        "v_cvt_pk_bf16_f32 v198, v52, v53\n\t"
        "v_cvt_pk_bf16_f32 v197, v38, v39\n\t"
        "v_mfma_f32_16x16x32_bf16 v[92:95], v[188:191], v[164:167], v[92:95]\n\t"
        "ds_read_b64_tr_b16 v[156:157], %[wa3] offset:16384\n\t"
        "v_cvt_pk_bf16_f32 v199, v54, v55\n\t"
        "v_permlane16_swap_b32 v196, v198\n\t"
        "v_mfma_f32_16x16x32_bf16 v[96:99], v[176:179], v[168:171], v[96:99]\n\t"
        "ds_read_b64_tr_b16 v[158:159], %[wa3] offset:17408\n\t"
        "v_permlane16_swap_b32 v197, v199\n\t"
        "buffer_store_dwordx4 v[196:199], %[so1], %[srd], 0 offen offset:512 sc1\n\t"
        "v_mfma_f32_16x16x32_bf16 v[100:103], v[180:183], v[168:171], v[100:103]\n\t"
        "ds_read_b128 v[132:135], %[xa0] offset:2048\n\t"
        "v_mul_f32 v40, %[sc], v40\n\t"
        "v_mul_f32 v41, %[sc], v41\n\t"
        "v_mfma_f32_16x16x32_bf16 v[104:107], v[184:187], v[168:171], v[104:107]\n\t"
        "ds_read_b128 v[136:139], %[xa0] offset:4096\n\t"
        "v_mul_f32 v42, %[sc], v42\n\t"
        "v_mul_f32 v43, %[sc], v43\n\t"
        "v_mfma_f32_16x16x32_bf16 v[108:111], v[188:191], v[168:171], v[108:111]\n\t"
        "ds_read_b128 v[140:143], %[xa0] offset:6144\n\t"
        "v_mul_f32 v56, %[sc], v56\n\t"
        "v_mul_f32 v57, %[sc], v57\n\t"
        "v_mfma_f32_16x16x32_bf16 v[112:115], v[176:179], v[172:175], v[112:115]\n\t"
        "v_mul_f32 v58, %[sc], v58\n\t"
        "v_mul_f32 v59, %[sc], v59\n\t"
        "v_mfma_f32_16x16x32_bf16 v[116:119], v[180:183], v[172:175], v[116:119]\n\t"
        "v_cvt_pk_bf16_f32 v192, v40, v41\n\t"
        "v_cvt_pk_bf16_f32 v194, v56, v57\n\t"
        "v_mfma_f32_16x16x32_bf16 v[120:123], v[184:187], v[172:175], v[120:123]\n\t"
        "v_cvt_pk_bf16_f32 v193, v42, v43\n\t"
        "v_cvt_pk_bf16_f32 v195, v58, v59\n\t"
        "v_mfma_f32_16x16x32_bf16 v[124:127], v[188:191], v[172:175], v[124:127]\n\t"
        "v_permlane16_swap_b32 v192, v194\n\t"
        "v_permlane16_swap_b32 v193, v195\n\t"
        "buffer_store_dwordx4 v[192:195], %[so2], %[srd], 0 offen offset:512 sc1\n\t"
        "v_mul_f32 v44, %[sc], v44\n\t"
        "v_mul_f32 v45, %[sc], v45\n\t"
        "v_mul_f32 v46, %[sc], v46\n\t"
        "v_mul_f32 v47, %[sc], v47\n\t"
        "v_mul_f32 v60, %[sc], v60\n\t"
        "v_mul_f32 v61, %[sc], v61\n\t"
        "v_mul_f32 v62, %[sc], v62\n\t"
        "v_mul_f32 v63, %[sc], v63\n\t"
        "v_cvt_pk_bf16_f32 v196, v44, v45\n\t"
        "v_cvt_pk_bf16_f32 v198, v60, v61\n\t"
        "v_cvt_pk_bf16_f32 v197, v46, v47\n\t"
        "v_cvt_pk_bf16_f32 v199, v62, v63\n\t"
        "v_permlane16_swap_b32 v196, v198\n\t"
        "s_nop 0\n\t"
        "v_permlane16_swap_b32 v197, v199\n\t"
        "buffer_store_dwordx4 v[196:199], %[so3], %[srd], 0 offen offset:512 sc1\n\t"
        "s_mov_b32 %[cnt], 2\n\t"
        "3:\n\t"
        "s_waitcnt lgkmcnt(3)\n\t"
        "v_mfma_f32_16x16x32_bf16 v[64:67], v[144:147], v[128:131], v[64:67]\n\t"
        "ds_read_b128 v[160:163], %[xa1] offset:0\n\t"
        "v_mfma_f32_16x16x32_bf16 v[68:71], v[148:151], v[128:131], v[68:71]\n\t"
        "ds_read_b64_tr_b16 v[176:177], %[wa0] offset:24576\n\t"
        "s_add_u32 %[t0], %[slot], 0x8000\n\t"
        "v_mfma_f32_16x16x32_bf16 v[72:75], v[152:155], v[128:131], v[72:75]\n\t"
        "ds_read_b64_tr_b16 v[178:179], %[wa0] offset:25600\n\t"
        "s_and_b32 %[t0], %[t0], 0x1ffff\n\t"
        "v_mfma_f32_16x16x32_bf16 v[76:79], v[156:159], v[128:131], v[76:79]\n\t"
        "ds_read_b64_tr_b16 v[180:181], %[wa1] offset:24576\n\t"
        "s_xor_b32 %[slot], %[slot], %[t0]\n\t"
        "s_waitcnt lgkmcnt(6)\n\t"
        "v_mfma_f32_16x16x32_bf16 v[80:83], v[144:147], v[132:135], v[80:83]\n\t"
        "ds_read_b64_tr_b16 v[182:183], %[wa1] offset:25600\n\t"
        "v_mfma_f32_16x16x32_bf16 v[84:87], v[148:151], v[132:135], v[84:87]\n\t"
        "ds_read_b64_tr_b16 v[184:185], %[wa2] offset:24576\n\t"
        "v_mfma_f32_16x16x32_bf16 v[88:91], v[152:155], v[132:135], v[88:91]\n\t"
        "ds_read_b64_tr_b16 v[186:187], %[wa2] offset:25600\n\t"
        "v_mfma_f32_16x16x32_bf16 v[92:95], v[156:159], v[132:135], v[92:95]\n\t"
        "ds_read_b64_tr_b16 v[188:189], %[wa3] offset:24576\n\t"
        "s_waitcnt lgkmcnt(9)\n\t"
        "v_mfma_f32_16x16x32_bf16 v[96:99], v[144:147], v[136:139], v[96:99]\n\t"
        "ds_read_b64_tr_b16 v[190:191], %[wa3] offset:25600\n\t"
        "v_mfma_f32_16x16x32_bf16 v[100:103], v[148:151], v[136:139], v[100:103]\n\t"
        "ds_read_b128 v[164:167], %[xa1] offset:2048\n\t"
        "v_mfma_f32_16x16x32_bf16 v[104:107], v[152:155], v[136:139], v[104:107]\n\t"
        "ds_read_b128 v[168:171], %[xa1] offset:4096\n\t"
        "v_mfma_f32_16x16x32_bf16 v[108:111], v[156:159], v[136:139], v[108:111]\n\t"
        "ds_read_b128 v[172:175], %[xa1] offset:6144\n\t"
        "s_waitcnt lgkmcnt(12)\n\t"
        "v_mfma_f32_16x16x32_bf16 v[112:115], v[144:147], v[140:143], v[112:115]\n\t"
        "v_xor_b32 %[xa0], %[slot], %[xa0]\n\t"
        "v_xor_b32 %[xa1], %[slot], %[xa1]\n\t"
        "v_mfma_f32_16x16x32_bf16 v[116:119], v[148:151], v[140:143], v[116:119]\n\t"
        "v_xor_b32 %[wa0], %[slot], %[wa0]\n\t"
        "v_xor_b32 %[wa1], %[slot], %[wa1]\n\t"
        "v_mfma_f32_16x16x32_bf16 v[120:123], v[152:155], v[140:143], v[120:123]\n\t"
        "v_xor_b32 %[wa2], %[slot], %[wa2]\n\t"
        "v_xor_b32 %[wa3], %[slot], %[wa3]\n\t"
        "v_mfma_f32_16x16x32_bf16 v[124:127], v[156:159], v[140:143], v[124:127]\n\t"
        "s_mov_b32 %[slot], %[t0]\n\t"
        "s_cmp_eq_u32 %[cnt], 9\n\t"
        "s_cbranch_scc1 5f\n\t"
        "s_add_u32 %[cnt], %[cnt], 1\n\t"
        "s_cmp_eq_u32 %[cnt], %[dr]\n\t"
        "s_cbranch_scc1 6f\n\t"
        "4:\n\t"
        "s_cmp_eq_u32 %[cnt], 9\n\t"
        "s_waitcnt lgkmcnt(0)\n\t"
        "s_cbranch_scc1 9f\n\t"
        "s_barrier\n\t"
        "9:\n\t"
        "v_mfma_f32_16x16x32_bf16 v[64:67], v[176:179], v[160:163], v[64:67]\n\t"
        "ds_read_b128 v[128:131], %[xa0] offset:0\n\t"
        "v_mfma_f32_16x16x32_bf16 v[68:71], v[180:183], v[160:163], v[68:71]\n\t"
        "ds_read_b64_tr_b16 v[144:145], %[wa0] offset:16384\n\t"
        "v_mfma_f32_16x16x32_bf16 v[72:75], v[184:187], v[160:163], v[72:75]\n\t"
        "ds_read_b64_tr_b16 v[146:147], %[wa0] offset:17408\n\t"
        "v_mfma_f32_16x16x32_bf16 v[76:79], v[188:191], v[160:163], v[76:79]\n\t"
        "ds_read_b64_tr_b16 v[148:149], %[wa1] offset:16384\n\t"
        "v_mfma_f32_16x16x32_bf16 v[80:83], v[176:179], v[164:167], v[80:83]\n\t"
        "ds_read_b64_tr_b16 v[150:151], %[wa1] offset:17408\n\t"
        "v_mfma_f32_16x16x32_bf16 v[84:87], v[180:183], v[164:167], v[84:87]\n\t"
        "ds_read_b64_tr_b16 v[152:153], %[wa2] offset:16384\n\t"
        "v_mfma_f32_16x16x32_bf16 v[88:91], v[184:187], v[164:167], v[88:91]\n\t"
        "ds_read_b64_tr_b16 v[154:155], %[wa2] offset:17408\n\t"
        "v_mfma_f32_16x16x32_bf16 v[92:95], v[188:191], v[164:167], v[92:95]\n\t"
        "ds_read_b64_tr_b16 v[156:157], %[wa3] offset:16384\n\t"
        "v_mfma_f32_16x16x32_bf16 v[96:99], v[176:179], v[168:171], v[96:99]\n\t"
        "ds_read_b64_tr_b16 v[158:159], %[wa3] offset:17408\n\t"
        "v_mfma_f32_16x16x32_bf16 v[100:103], v[180:183], v[168:171], v[100:103]\n\t"
        "ds_read_b128 v[132:135], %[xa0] offset:2048\n\t"
        "v_mfma_f32_16x16x32_bf16 v[104:107], v[184:187], v[168:171], v[104:107]\n\t"
        "ds_read_b128 v[136:139], %[xa0] offset:4096\n\t"
        "v_mfma_f32_16x16x32_bf16 v[108:111], v[188:191], v[168:171], v[108:111]\n\t"
        "ds_read_b128 v[140:143], %[xa0] offset:6144\n\t"
        "v_mfma_f32_16x16x32_bf16 v[112:115], v[176:179], v[172:175], v[112:115]\n\t"
        "v_mfma_f32_16x16x32_bf16 v[116:119], v[180:183], v[172:175], v[116:119]\n\t"
        "v_mfma_f32_16x16x32_bf16 v[120:123], v[184:187], v[172:175], v[120:123]\n\t"
        "v_mfma_f32_16x16x32_bf16 v[124:127], v[188:191], v[172:175], v[124:127]\n\t"
        "s_branch 3b\n\t"
        "6:\n\t"
        "s_waitcnt vmcnt(0)\n\t"
        "s_branch 4b\n\t"
        "5:\n\t"
        "s_waitcnt lgkmcnt(0)\n\t"
        "v_mfma_f32_16x16x32_bf16 v[64:67], v[176:179], v[160:163], v[64:67]\n\t"
        "v_mfma_f32_16x16x32_bf16 v[68:71], v[180:183], v[160:163], v[68:71]\n\t"
        "v_mfma_f32_16x16x32_bf16 v[72:75], v[184:187], v[160:163], v[72:75]\n\t"
        "v_mfma_f32_16x16x32_bf16 v[76:79], v[188:191], v[160:163], v[76:79]\n\t"
        "v_mfma_f32_16x16x32_bf16 v[80:83], v[176:179], v[164:167], v[80:83]\n\t"
        "v_mfma_f32_16x16x32_bf16 v[84:87], v[180:183], v[164:167], v[84:87]\n\t"
        "v_mfma_f32_16x16x32_bf16 v[88:91], v[184:187], v[164:167], v[88:91]\n\t"
        "v_mfma_f32_16x16x32_bf16 v[92:95], v[188:191], v[164:167], v[92:95]\n\t"
        "v_mfma_f32_16x16x32_bf16 v[96:99], v[176:179], v[168:171], v[96:99]\n\t"
        "v_mfma_f32_16x16x32_bf16 v[100:103], v[180:183], v[168:171], v[100:103]\n\t"
        "v_mfma_f32_16x16x32_bf16 v[104:107], v[184:187], v[168:171], v[104:107]\n\t"
        "v_mfma_f32_16x16x32_bf16 v[108:111], v[188:191], v[168:171], v[108:111]\n\t"
        "v_mfma_f32_16x16x32_bf16 v[112:115], v[176:179], v[172:175], v[112:115]\n\t"
        "v_mfma_f32_16x16x32_bf16 v[116:119], v[180:183], v[172:175], v[116:119]\n\t"
        "v_mfma_f32_16x16x32_bf16 v[120:123], v[184:187], v[172:175], v[120:123]\n\t"
        "v_mfma_f32_16x16x32_bf16 v[124:127], v[188:191], v[172:175], v[124:127]\n\t"
        "s_nop 7\n\t"
        GEMM_OPERANDS());
}
#define ONES_OPERANDS , [on] "v"(C.on), [srd0] "s"(srd0)
DI void gemm_xw(Comp& C, u32x4 srd, u32x4 srd0, float sc, unsigned dr, f32x32& a0, f32x32& a1) {
    unsigned cnt = 10, slot = 0, t0;
    asm volatile(
        "s_barrier\n\t"
        "ds_read_b128 v[128:131], %[xa0] offset:0\n\t"
        "ds_read_b64_tr_b16 v[144:145], %[wa0] offset:16384\n\t"
        "ds_read_b64_tr_b16 v[146:147], %[wa0] offset:17408\n\t"
        "ds_read_b64_tr_b16 v[148:149], %[wa1] offset:16384\n\t"
        "ds_read_b64_tr_b16 v[150:151], %[wa1] offset:17408\n\t"
        "ds_read_b64_tr_b16 v[152:153], %[wa2] offset:16384\n\t"
        "ds_read_b64_tr_b16 v[154:155], %[wa2] offset:17408\n\t"
        "ds_read_b64_tr_b16 v[156:157], %[wa3] offset:16384\n\t"
        "ds_read_b64_tr_b16 v[158:159], %[wa3] offset:17408\n\t"
        "ds_read_b128 v[132:135], %[xa0] offset:2048\n\t"
        "ds_read_b128 v[136:139], %[xa0] offset:4096\n\t"
        "ds_read_b128 v[140:143], %[xa0] offset:6144\n\t"
        "s_waitcnt lgkmcnt(3)\n\t"
        "v_mfma_f32_16x16x32_bf16 v[0:3], v[128:131], v[144:147], 0\n\t"
        "ds_read_b128 v[160:163], %[xa1] offset:0\n\t"
        "v_mfma_f32_16x16x32_bf16 v[4:7], v[128:131], v[148:151], 0\n\t"
        "ds_read_b64_tr_b16 v[176:177], %[wa0] offset:24576\n\t"
        "s_add_u32 %[t0], %[slot], 0x8000\n\t"
        "v_mfma_f32_16x16x32_bf16 v[8:11], v[128:131], v[152:155], 0\n\t"
        "ds_read_b64_tr_b16 v[178:179], %[wa0] offset:25600\n\t"
        "s_and_b32 %[t0], %[t0], 0x1ffff\n\t"
        "v_mfma_f32_16x16x32_bf16 v[12:15], v[128:131], v[156:159], 0\n\t"
        "ds_read_b64_tr_b16 v[180:181], %[wa1] offset:24576\n\t"
        "s_xor_b32 %[slot], %[slot], %[t0]\n\t"
        "s_waitcnt lgkmcnt(6)\n\t"
        "v_mfma_f32_16x16x32_bf16 v[16:19], v[132:135], v[144:147], 0\n\t"
        "ds_read_b64_tr_b16 v[182:183], %[wa1] offset:25600\n\t"
        "v_mfma_f32_16x16x32_bf16 v[20:23], v[132:135], v[148:151], 0\n\t"
        "ds_read_b64_tr_b16 v[184:185], %[wa2] offset:24576\n\t"
        "v_mfma_f32_16x16x32_bf16 v[24:27], v[132:135], v[152:155], 0\n\t"
        "ds_read_b64_tr_b16 v[186:187], %[wa2] offset:25600\n\t"
        "v_mfma_f32_16x16x32_bf16 v[28:31], v[132:135], v[156:159], 0\n\t"
        "ds_read_b64_tr_b16 v[188:189], %[wa3] offset:24576\n\t"
        "s_waitcnt lgkmcnt(9)\n\t"
        "v_mfma_f32_16x16x32_bf16 v[32:35], v[136:139], v[144:147], 0\n\t"
        "ds_read_b64_tr_b16 v[190:191], %[wa3] offset:25600\n\t"
        "v_mfma_f32_16x16x32_bf16 v[36:39], v[136:139], v[148:151], 0\n\t"
        "ds_read_b128 v[164:167], %[xa1] offset:2048\n\t"
        "v_mfma_f32_16x16x32_bf16 v[40:43], v[136:139], v[152:155], 0\n\t"
        "ds_read_b128 v[168:171], %[xa1] offset:4096\n\t"
        "v_mfma_f32_16x16x32_bf16 v[44:47], v[136:139], v[156:159], 0\n\t"
        "ds_read_b128 v[172:175], %[xa1] offset:6144\n\t"
        "s_waitcnt lgkmcnt(12)\n\t"
        "v_mfma_f32_16x16x32_bf16 v[48:51], v[140:143], v[144:147], 0\n\t"
        "v_xor_b32 %[xa0], %[slot], %[xa0]\n\t"
        "v_xor_b32 %[xa1], %[slot], %[xa1]\n\t"
        "v_mfma_f32_16x16x32_bf16 v[52:55], v[140:143], v[148:151], 0\n\t"
        "v_xor_b32 %[wa0], %[slot], %[wa0]\n\t"
        "v_xor_b32 %[wa1], %[slot], %[wa1]\n\t"
        "v_mfma_f32_16x16x32_bf16 v[56:59], v[140:143], v[152:155], 0\n\t"
        "v_xor_b32 %[wa2], %[slot], %[wa2]\n\t"
        "v_xor_b32 %[wa3], %[slot], %[wa3]\n\t"
        "v_mfma_f32_16x16x32_bf16 v[60:63], v[140:143], v[156:159], 0\n\t"
        "s_mov_b32 %[slot], %[t0]\n\t"
        "s_branch 2f\n\t"
        "1:\n\t"
        "s_waitcnt lgkmcnt(3)\n\t"
        "v_mfma_f32_16x16x32_bf16 v[0:3], v[128:131], v[144:147], v[0:3]\n\t"
        "ds_read_b128 v[160:163], %[xa1] offset:0\n\t"
        "v_mfma_f32_16x16x32_bf16 v[4:7], v[128:131], v[148:151], v[4:7]\n\t"
        "ds_read_b64_tr_b16 v[176:177], %[wa0] offset:24576\n\t"
        "s_add_u32 %[t0], %[slot], 0x8000\n\t"
        "v_mfma_f32_16x16x32_bf16 v[8:11], v[128:131], v[152:155], v[8:11]\n\t"
        "ds_read_b64_tr_b16 v[178:179], %[wa0] offset:25600\n\t"
        "s_and_b32 %[t0], %[t0], 0x1ffff\n\t"
        "v_mfma_f32_16x16x32_bf16 v[12:15], v[128:131], v[156:159], v[12:15]\n\t"
        "ds_read_b64_tr_b16 v[180:181], %[wa1] offset:24576\n\t"
        "s_xor_b32 %[slot], %[slot], %[t0]\n\t"
        "s_waitcnt lgkmcnt(6)\n\t"
        "v_mfma_f32_16x16x32_bf16 v[16:19], v[132:135], v[144:147], v[16:19]\n\t"
        "ds_read_b64_tr_b16 v[182:183], %[wa1] offset:25600\n\t"
        "v_mfma_f32_16x16x32_bf16 v[20:23], v[132:135], v[148:151], v[20:23]\n\t"
        "ds_read_b64_tr_b16 v[184:185], %[wa2] offset:24576\n\t"
        "v_mfma_f32_16x16x32_bf16 v[24:27], v[132:135], v[152:155], v[24:27]\n\t"
        "ds_read_b64_tr_b16 v[186:187], %[wa2] offset:25600\n\t"
        "v_mfma_f32_16x16x32_bf16 v[28:31], v[132:135], v[156:159], v[28:31]\n\t"
        "ds_read_b64_tr_b16 v[188:189], %[wa3] offset:24576\n\t"
        "s_waitcnt lgkmcnt(9)\n\t"
        "v_mfma_f32_16x16x32_bf16 v[32:35], v[136:139], v[144:147], v[32:35]\n\t"
        "ds_read_b64_tr_b16 v[190:191], %[wa3] offset:25600\n\t"
        "v_mfma_f32_16x16x32_bf16 v[36:39], v[136:139], v[148:151], v[36:39]\n\t"
        "ds_read_b128 v[164:167], %[xa1] offset:2048\n\t"
        "v_mfma_f32_16x16x32_bf16 v[40:43], v[136:139], v[152:155], v[40:43]\n\t"
        "ds_read_b128 v[168:171], %[xa1] offset:4096\n\t"
        "v_mfma_f32_16x16x32_bf16 v[44:47], v[136:139], v[156:159], v[44:47]\n\t"
        "ds_read_b128 v[172:175], %[xa1] offset:6144\n\t"
        "s_waitcnt lgkmcnt(12)\n\t"
        "v_mfma_f32_16x16x32_bf16 v[48:51], v[140:143], v[144:147], v[48:51]\n\t"
        "v_xor_b32 %[xa0], %[slot], %[xa0]\n\t"
        "v_xor_b32 %[xa1], %[slot], %[xa1]\n\t"
        "v_mfma_f32_16x16x32_bf16 v[52:55], v[140:143], v[148:151], v[52:55]\n\t"
        "v_xor_b32 %[wa0], %[slot], %[wa0]\n\t"
        "v_xor_b32 %[wa1], %[slot], %[wa1]\n\t"
        "v_mfma_f32_16x16x32_bf16 v[56:59], v[140:143], v[152:155], v[56:59]\n\t"
        "v_xor_b32 %[wa2], %[slot], %[wa2]\n\t"
        "v_xor_b32 %[wa3], %[slot], %[wa3]\n\t"
        "v_mfma_f32_16x16x32_bf16 v[60:63], v[140:143], v[156:159], v[60:63]\n\t"
        "s_mov_b32 %[slot], %[t0]\n\t"
        "2:\n\t"
        "s_waitcnt lgkmcnt(0)\n\t"
        "s_barrier\n\t"
        "v_mfma_f32_16x16x32_bf16 v[0:3], v[160:163], v[176:179], v[0:3]\n\t"
        "ds_read_b128 v[128:131], %[xa0] offset:0\n\t"
        "v_mfma_f32_16x16x32_bf16 v[4:7], v[160:163], v[180:183], v[4:7]\n\t"
        "ds_read_b64_tr_b16 v[144:145], %[wa0] offset:16384\n\t"
        "v_mfma_f32_16x16x32_bf16 v[8:11], v[160:163], v[184:187], v[8:11]\n\t"
        "ds_read_b64_tr_b16 v[146:147], %[wa0] offset:17408\n\t"
        "v_mfma_f32_16x16x32_bf16 v[12:15], v[160:163], v[188:191], v[12:15]\n\t"
        "ds_read_b64_tr_b16 v[148:149], %[wa1] offset:16384\n\t"
        "v_mfma_f32_16x16x32_bf16 v[16:19], v[164:167], v[176:179], v[16:19]\n\t"
        "ds_read_b64_tr_b16 v[150:151], %[wa1] offset:17408\n\t"
        "v_mfma_f32_16x16x32_bf16 v[20:23], v[164:167], v[180:183], v[20:23]\n\t"
        "ds_read_b64_tr_b16 v[152:153], %[wa2] offset:16384\n\t"
        "v_mfma_f32_16x16x32_bf16 v[24:27], v[164:167], v[184:187], v[24:27]\n\t"
        "ds_read_b64_tr_b16 v[154:155], %[wa2] offset:17408\n\t"
        "v_mfma_f32_16x16x32_bf16 v[28:31], v[164:167], v[188:191], v[28:31]\n\t"
        "ds_read_b64_tr_b16 v[156:157], %[wa3] offset:16384\n\t"
        "v_mfma_f32_16x16x32_bf16 v[32:35], v[168:171], v[176:179], v[32:35]\n\t"
        "ds_read_b64_tr_b16 v[158:159], %[wa3] offset:17408\n\t"
        "v_mfma_f32_16x16x32_bf16 v[36:39], v[168:171], v[180:183], v[36:39]\n\t"
        "ds_read_b128 v[132:135], %[xa0] offset:2048\n\t"
        "v_mfma_f32_16x16x32_bf16 v[40:43], v[168:171], v[184:187], v[40:43]\n\t"
        "ds_read_b128 v[136:139], %[xa0] offset:4096\n\t"
        "v_mfma_f32_16x16x32_bf16 v[44:47], v[168:171], v[188:191], v[44:47]\n\t"
        "ds_read_b128 v[140:143], %[xa0] offset:6144\n\t"
        "v_mfma_f32_16x16x32_bf16 v[48:51], v[172:175], v[176:179], v[48:51]\n\t"
        "v_mfma_f32_16x16x32_bf16 v[52:55], v[172:175], v[180:183], v[52:55]\n\t"
        "v_mfma_f32_16x16x32_bf16 v[56:59], v[172:175], v[184:187], v[56:59]\n\t"
        "v_mfma_f32_16x16x32_bf16 v[60:63], v[172:175], v[188:191], v[60:63]\n\t"
        "s_cmp_eq_u32 %[cnt], 6\n\t"
        "s_cbranch_scc1 7f\n\t"
        "8:\n\t"
        "s_sub_u32 %[cnt], %[cnt], 1\n\t"
        "s_cmp_lg_u32 %[cnt], 0\n\t"
        "s_cbranch_scc1 1b\n\t"
        "s_waitcnt lgkmcnt(3)\n\t"
        "v_mfma_f32_16x16x32_bf16 v[64:67], v[128:131], v[144:147], 0\n\t"
        "ds_read_b128 v[160:163], %[xa1] offset:0\n\t"
        "v_mfma_f32_16x16x32_bf16 v[68:71], v[128:131], v[148:151], 0\n\t"
        "ds_read_b64_tr_b16 v[176:177], %[wa0] offset:24576\n\t"
        "s_add_u32 %[t0], %[slot], 0x8000\n\t"
        "v_mfma_f32_16x16x32_bf16 v[72:75], v[128:131], v[152:155], 0\n\t"
        "ds_read_b64_tr_b16 v[178:179], %[wa0] offset:25600\n\t"
        "s_and_b32 %[t0], %[t0], 0x1ffff\n\t"
        "v_mfma_f32_16x16x32_bf16 v[76:79], v[128:131], v[156:159], 0\n\t"
        "ds_read_b64_tr_b16 v[180:181], %[wa1] offset:24576\n\t"
        "s_xor_b32 %[slot], %[slot], %[t0]\n\t"
        "s_waitcnt lgkmcnt(6)\n\t"
        "v_mfma_f32_16x16x32_bf16 v[80:83], v[132:135], v[144:147], 0\n\t"
        "ds_read_b64_tr_b16 v[182:183], %[wa1] offset:25600\n\t"
        "v_cvt_pk_bf16_f32 v192, v0, v1\n\t"
        "v_cvt_pk_bf16_f32 v194, v16, v17\n\t"
        "v_mfma_f32_16x16x32_bf16 v[84:87], v[132:135], v[148:151], 0\n\t"
        "ds_read_b64_tr_b16 v[184:185], %[wa2] offset:24576\n\t"
        "v_cvt_pk_bf16_f32 v193, v2, v3\n\t"
        "v_cvt_pk_bf16_f32 v195, v18, v19\n\t"
        "v_mfma_f32_16x16x32_bf16 v[88:91], v[132:135], v[152:155], 0\n\t"
        "ds_read_b64_tr_b16 v[186:187], %[wa2] offset:25600\n\t"
        "buffer_store_dwordx4 v[192:195], %[so0], %[srd], 0 offen offset:0 sc1\n\t"
        "v_cvt_pk_bf16_f32 v196, v4, v5\n\t"
        "v_mfma_f32_16x16x32_bf16 v[92:95], v[132:135], v[156:159], 0\n\t"
        "ds_read_b64_tr_b16 v[188:189], %[wa3] offset:24576\n\t"
        "v_cvt_pk_bf16_f32 v198, v20, v21\n\t"
        "v_cvt_pk_bf16_f32 v197, v6, v7\n\t"
        "s_waitcnt lgkmcnt(9)\n\t"
        "v_mfma_f32_16x16x32_bf16 v[96:99], v[136:139], v[144:147], 0\n\t"
        "ds_read_b64_tr_b16 v[190:191], %[wa3] offset:25600\n\t"
        "v_cvt_pk_bf16_f32 v199, v22, v23\n\t"
        "buffer_store_dwordx4 v[196:199], %[so0], %[srd], 0 offen offset:256 sc1\n\t"
        "v_mfma_f32_16x16x32_bf16 v[100:103], v[136:139], v[148:151], 0\n\t"
        "ds_read_b128 v[164:167], %[xa1] offset:2048\n\t"
        "v_cvt_pk_bf16_f32 v192, v8, v9\n\t"
        "v_cvt_pk_bf16_f32 v194, v24, v25\n\t"
        "v_mfma_f32_16x16x32_bf16 v[104:107], v[136:139], v[152:155], 0\n\t"
        "ds_read_b128 v[168:171], %[xa1] offset:4096\n\t"
        "v_cvt_pk_bf16_f32 v193, v10, v11\n\t"
        "v_cvt_pk_bf16_f32 v195, v26, v27\n\t"
        "v_mfma_f32_16x16x32_bf16 v[108:111], v[136:139], v[156:159], 0\n\t"
        "ds_read_b128 v[172:175], %[xa1] offset:6144\n\t"
        "buffer_store_dwordx4 v[192:195], %[so0], %[srd], 0 offen offset:512 sc1\n\t"
        "v_cvt_pk_bf16_f32 v196, v12, v13\n\t"
        "s_waitcnt lgkmcnt(12)\n\t"
        "v_mfma_f32_16x16x32_bf16 v[112:115], v[140:143], v[144:147], 0\n\t"
        "v_xor_b32 %[xa0], %[slot], %[xa0]\n\t"
        "v_xor_b32 %[xa1], %[slot], %[xa1]\n\t"
        "v_mfma_f32_16x16x32_bf16 v[116:119], v[140:143], v[148:151], 0\n\t"
        "v_xor_b32 %[wa0], %[slot], %[wa0]\n\t"
        "v_xor_b32 %[wa1], %[slot], %[wa1]\n\t"
        "v_mfma_f32_16x16x32_bf16 v[120:123], v[140:143], v[152:155], 0\n\t"
        "v_xor_b32 %[wa2], %[slot], %[wa2]\n\t"
        "v_xor_b32 %[wa3], %[slot], %[wa3]\n\t"
        "v_mfma_f32_16x16x32_bf16 v[124:127], v[140:143], v[156:159], 0\n\t"
        "s_mov_b32 %[slot], %[t0]\n\t"
        "s_waitcnt lgkmcnt(0)\n\t"
        "s_barrier\n\t"
        "v_mfma_f32_16x16x32_bf16 v[64:67], v[160:163], v[176:179], v[64:67]\n\t"
        "ds_read_b128 v[128:131], %[xa0] offset:0\n\t"
        "v_cvt_pk_bf16_f32 v198, v28, v29\n\t"
        "v_cvt_pk_bf16_f32 v197, v14, v15\n\t"
        "v_mfma_f32_16x16x32_bf16 v[68:71], v[160:163], v[180:183], v[68:71]\n\t"
        "ds_read_b64_tr_b16 v[144:145], %[wa0] offset:16384\n\t"
        "v_cvt_pk_bf16_f32 v199, v30, v31\n\t"
        "buffer_store_dwordx4 v[196:199], %[so0], %[srd], 0 offen offset:768 sc1\n\t"
        "v_mfma_f32_16x16x32_bf16 v[72:75], v[160:163], v[184:187], v[72:75]\n\t"
        "ds_read_b64_tr_b16 v[146:147], %[wa0] offset:17408\n\t"
        "v_cvt_pk_bf16_f32 v192, v32, v33\n\t"
        "v_cvt_pk_bf16_f32 v194, v48, v49\n\t"
        "v_mfma_f32_16x16x32_bf16 v[76:79], v[160:163], v[188:191], v[76:79]\n\t"
        "ds_read_b64_tr_b16 v[148:149], %[wa1] offset:16384\n\t"
        "v_cvt_pk_bf16_f32 v193, v34, v35\n\t"
        "v_cvt_pk_bf16_f32 v195, v50, v51\n\t"
        "v_mfma_f32_16x16x32_bf16 v[80:83], v[164:167], v[176:179], v[80:83]\n\t"
        "ds_read_b64_tr_b16 v[150:151], %[wa1] offset:17408\n\t"
        "buffer_store_dwordx4 v[192:195], %[so1], %[srd], 0 offen offset:0 sc1\n\t"
        "v_cvt_pk_bf16_f32 v196, v36, v37\n\t"
        "v_mfma_f32_16x16x32_bf16 v[84:87], v[164:167], v[180:183], v[84:87]\n\t"
        "ds_read_b64_tr_b16 v[152:153], %[wa2] offset:16384\n\t"
        "v_cvt_pk_bf16_f32 v198, v52, v53\n\t"
        "v_cvt_pk_bf16_f32 v197, v38, v39\n\t"
        "v_mfma_f32_16x16x32_bf16 v[88:91], v[164:167], v[184:187], v[88:91]\n\t"
        "ds_read_b64_tr_b16 v[154:155], %[wa2] offset:17408\n\t"
        "v_cvt_pk_bf16_f32 v199, v54, v55\n\t"
        "buffer_store_dwordx4 v[196:199], %[so1], %[srd], 0 offen offset:256 sc1\n\t"
        "v_mfma_f32_16x16x32_bf16 v[92:95], v[164:167], v[188:191], v[92:95]\n\t"
        "ds_read_b64_tr_b16 v[156:157], %[wa3] offset:16384\n\t"
        "v_cvt_pk_bf16_f32 v192, v40, v41\n\t"
        "v_cvt_pk_bf16_f32 v194, v56, v57\n\t"
        "v_mfma_f32_16x16x32_bf16 v[96:99], v[168:171], v[176:179], v[96:99]\n\t"
        "ds_read_b64_tr_b16 v[158:159], %[wa3] offset:17408\n\t"
        "v_cvt_pk_bf16_f32 v193, v42, v43\n\t"
        "v_cvt_pk_bf16_f32 v195, v58, v59\n\t"
        "v_mfma_f32_16x16x32_bf16 v[100:103], v[168:171], v[180:183], v[100:103]\n\t"
        "ds_read_b128 v[132:135], %[xa0] offset:2048\n\t"
        "buffer_store_dwordx4 v[192:195], %[so1], %[srd], 0 offen offset:512 sc1\n\t"
        "v_cvt_pk_bf16_f32 v196, v44, v45\n\t"
        "v_mfma_f32_16x16x32_bf16 v[104:107], v[168:171], v[184:187], v[104:107]\n\t"
        "ds_read_b128 v[136:139], %[xa0] offset:4096\n\t"
        "v_cvt_pk_bf16_f32 v198, v60, v61\n\t"
        "v_cvt_pk_bf16_f32 v197, v46, v47\n\t"
        "v_mfma_f32_16x16x32_bf16 v[108:111], v[168:171], v[188:191], v[108:111]\n\t"
        "ds_read_b128 v[140:143], %[xa0] offset:6144\n\t"
        "v_cvt_pk_bf16_f32 v199, v62, v63\n\t"
        "buffer_store_dwordx4 v[196:199], %[so1], %[srd], 0 offen offset:768 sc1\n\t"
        "v_mfma_f32_16x16x32_bf16 v[112:115], v[172:175], v[176:179], v[112:115]\n\t"
        "v_mfma_f32_16x16x32_bf16 v[116:119], v[172:175], v[180:183], v[116:119]\n\t"
        "v_mfma_f32_16x16x32_bf16 v[120:123], v[172:175], v[184:187], v[120:123]\n\t"
        "v_mfma_f32_16x16x32_bf16 v[124:127], v[172:175], v[188:191], v[124:127]\n\t"
        "s_waitcnt lgkmcnt(3)\n\t"
        "v_mfma_f32_16x16x32_bf16 v[64:67], v[128:131], v[144:147], v[64:67]\n\t"
        "ds_read_b128 v[160:163], %[xa1] offset:0\n\t"
        "v_mfma_f32_16x16x32_bf16 v[68:71], v[128:131], v[148:151], v[68:71]\n\t"
        "ds_read_b64_tr_b16 v[176:177], %[wa0] offset:24576\n\t"
        "s_add_u32 %[t0], %[slot], 0x8000\n\t"
        "v_mfma_f32_16x16x32_bf16 v[72:75], v[128:131], v[152:155], v[72:75]\n\t"
        "ds_read_b64_tr_b16 v[178:179], %[wa0] offset:25600\n\t"
        "s_and_b32 %[t0], %[t0], 0x1ffff\n\t"
        "v_mfma_f32_16x16x32_bf16 v[76:79], v[128:131], v[156:159], v[76:79]\n\t"
        "ds_read_b64_tr_b16 v[180:181], %[wa1] offset:24576\n\t"
        "s_xor_b32 %[slot], %[slot], %[t0]\n\t"
        "s_waitcnt lgkmcnt(6)\n\t"
        "v_mfma_f32_16x16x32_bf16 v[80:83], v[132:135], v[144:147], v[80:83]\n\t"
        "ds_read_b64_tr_b16 v[182:183], %[wa1] offset:25600\n\t"
        "v_mfma_f32_16x16x32_bf16 v[84:87], v[132:135], v[148:151], v[84:87]\n\t"
        "ds_read_b64_tr_b16 v[184:185], %[wa2] offset:24576\n\t"
        "v_mfma_f32_16x16x32_bf16 v[88:91], v[132:135], v[152:155], v[88:91]\n\t"
        "ds_read_b64_tr_b16 v[186:187], %[wa2] offset:25600\n\t"
        "v_mfma_f32_16x16x32_bf16 v[92:95], v[132:135], v[156:159], v[92:95]\n\t"
        "ds_read_b64_tr_b16 v[188:189], %[wa3] offset:24576\n\t"
        "s_waitcnt lgkmcnt(9)\n\t"
        "v_mfma_f32_16x16x32_bf16 v[96:99], v[136:139], v[144:147], v[96:99]\n\t"
        "ds_read_b64_tr_b16 v[190:191], %[wa3] offset:25600\n\t"
        "v_mfma_f32_16x16x32_bf16 v[100:103], v[136:139], v[148:151], v[100:103]\n\t"
        "ds_read_b128 v[164:167], %[xa1] offset:2048\n\t"
        "v_mfma_f32_16x16x32_bf16 v[104:107], v[136:139], v[152:155], v[104:107]\n\t"
        "ds_read_b128 v[168:171], %[xa1] offset:4096\n\t"
        "v_mfma_f32_16x16x32_bf16 v[108:111], v[136:139], v[156:159], v[108:111]\n\t"
        "ds_read_b128 v[172:175], %[xa1] offset:6144\n\t"
        "s_waitcnt lgkmcnt(12)\n\t"
        "v_mfma_f32_16x16x32_bf16 v[112:115], v[140:143], v[144:147], v[112:115]\n\t"
        "v_xor_b32 %[xa0], %[slot], %[xa0]\n\t"
        "v_xor_b32 %[xa1], %[slot], %[xa1]\n\t"
        "v_mfma_f32_16x16x32_bf16 v[116:119], v[140:143], v[148:151], v[116:119]\n\t"
        "v_xor_b32 %[wa0], %[slot], %[wa0]\n\t"
        "v_xor_b32 %[wa1], %[slot], %[wa1]\n\t"
        "v_mfma_f32_16x16x32_bf16 v[120:123], v[140:143], v[152:155], v[120:123]\n\t"
        "v_xor_b32 %[wa2], %[slot], %[wa2]\n\t"
        "v_xor_b32 %[wa3], %[slot], %[wa3]\n\t"
        "v_mfma_f32_16x16x32_bf16 v[124:127], v[140:143], v[156:159], v[124:127]\n\t"
        "s_mov_b32 %[slot], %[t0]\n\t"
        "s_waitcnt lgkmcnt(0)\n\t"
        "s_barrier\n\t"
        "v_mfma_f32_16x16x32_bf16 v[64:67], v[160:163], v[176:179], v[64:67]\n\t"
        "ds_read_b128 v[128:131], %[xa0] offset:0\n\t"
        "v_mfma_f32_16x16x32_bf16 v[68:71], v[160:163], v[180:183], v[68:71]\n\t"
        "ds_read_b64_tr_b16 v[144:145], %[wa0] offset:16384\n\t"
        "v_mfma_f32_16x16x32_bf16 v[72:75], v[160:163], v[184:187], v[72:75]\n\t"
        "ds_read_b64_tr_b16 v[146:147], %[wa0] offset:17408\n\t"
        "v_mfma_f32_16x16x32_bf16 v[76:79], v[160:163], v[188:191], v[76:79]\n\t"
        "ds_read_b64_tr_b16 v[148:149], %[wa1] offset:16384\n\t"
        "v_mfma_f32_16x16x32_bf16 v[80:83], v[164:167], v[176:179], v[80:83]\n\t"
        "ds_read_b64_tr_b16 v[150:151], %[wa1] offset:17408\n\t"
        "v_mfma_f32_16x16x32_bf16 v[84:87], v[164:167], v[180:183], v[84:87]\n\t"
        "ds_read_b64_tr_b16 v[152:153], %[wa2] offset:16384\n\t"
        "v_mfma_f32_16x16x32_bf16 v[88:91], v[164:167], v[184:187], v[88:91]\n\t"
        "ds_read_b64_tr_b16 v[154:155], %[wa2] offset:17408\n\t"
        "v_mfma_f32_16x16x32_bf16 v[92:95], v[164:167], v[188:191], v[92:95]\n\t"
        "ds_read_b64_tr_b16 v[156:157], %[wa3] offset:16384\n\t"
        "v_mfma_f32_16x16x32_bf16 v[96:99], v[168:171], v[176:179], v[96:99]\n\t"
        "ds_read_b64_tr_b16 v[158:159], %[wa3] offset:17408\n\t"
        "v_mfma_f32_16x16x32_bf16 v[100:103], v[168:171], v[180:183], v[100:103]\n\t"
        "ds_read_b128 v[132:135], %[xa0] offset:2048\n\t"
        "v_mfma_f32_16x16x32_bf16 v[104:107], v[168:171], v[184:187], v[104:107]\n\t"
        "ds_read_b128 v[136:139], %[xa0] offset:4096\n\t"
        "v_mfma_f32_16x16x32_bf16 v[108:111], v[168:171], v[188:191], v[108:111]\n\t"
        "ds_read_b128 v[140:143], %[xa0] offset:6144\n\t"
        "v_mfma_f32_16x16x32_bf16 v[112:115], v[172:175], v[176:179], v[112:115]\n\t"
        "v_mfma_f32_16x16x32_bf16 v[116:119], v[172:175], v[180:183], v[116:119]\n\t"
        "v_mfma_f32_16x16x32_bf16 v[120:123], v[172:175], v[184:187], v[120:123]\n\t"
        "v_mfma_f32_16x16x32_bf16 v[124:127], v[172:175], v[188:191], v[124:127]\n\t"
        "s_mov_b32 %[cnt], 2\n\t"
        "3:\n\t"
        "s_waitcnt lgkmcnt(3)\n\t"
        "v_mfma_f32_16x16x32_bf16 v[64:67], v[128:131], v[144:147], v[64:67]\n\t"
        "ds_read_b128 v[160:163], %[xa1] offset:0\n\t"
        "v_mfma_f32_16x16x32_bf16 v[68:71], v[128:131], v[148:151], v[68:71]\n\t"
        "ds_read_b64_tr_b16 v[176:177], %[wa0] offset:24576\n\t"
        "s_add_u32 %[t0], %[slot], 0x8000\n\t"
        "v_mfma_f32_16x16x32_bf16 v[72:75], v[128:131], v[152:155], v[72:75]\n\t"
        "ds_read_b64_tr_b16 v[178:179], %[wa0] offset:25600\n\t"
        "s_and_b32 %[t0], %[t0], 0x1ffff\n\t"
        "v_mfma_f32_16x16x32_bf16 v[76:79], v[128:131], v[156:159], v[76:79]\n\t"
        "ds_read_b64_tr_b16 v[180:181], %[wa1] offset:24576\n\t"
        "s_xor_b32 %[slot], %[slot], %[t0]\n\t"
        "s_waitcnt lgkmcnt(6)\n\t"
        "v_mfma_f32_16x16x32_bf16 v[80:83], v[132:135], v[144:147], v[80:83]\n\t"
        "ds_read_b64_tr_b16 v[182:183], %[wa1] offset:25600\n\t"
        "v_mfma_f32_16x16x32_bf16 v[84:87], v[132:135], v[148:151], v[84:87]\n\t"
        "ds_read_b64_tr_b16 v[184:185], %[wa2] offset:24576\n\t"
        "v_mfma_f32_16x16x32_bf16 v[88:91], v[132:135], v[152:155], v[88:91]\n\t"
        "ds_read_b64_tr_b16 v[186:187], %[wa2] offset:25600\n\t"
        "v_mfma_f32_16x16x32_bf16 v[92:95], v[132:135], v[156:159], v[92:95]\n\t"
        "ds_read_b64_tr_b16 v[188:189], %[wa3] offset:24576\n\t"
        "s_waitcnt lgkmcnt(9)\n\t"
        "v_mfma_f32_16x16x32_bf16 v[96:99], v[136:139], v[144:147], v[96:99]\n\t"
        "ds_read_b64_tr_b16 v[190:191], %[wa3] offset:25600\n\t"
        "v_mfma_f32_16x16x32_bf16 v[100:103], v[136:139], v[148:151], v[100:103]\n\t"
        "ds_read_b128 v[164:167], %[xa1] offset:2048\n\t"
        "v_mfma_f32_16x16x32_bf16 v[104:107], v[136:139], v[152:155], v[104:107]\n\t"
        "ds_read_b128 v[168:171], %[xa1] offset:4096\n\t"
        "v_mfma_f32_16x16x32_bf16 v[108:111], v[136:139], v[156:159], v[108:111]\n\t"
        "ds_read_b128 v[172:175], %[xa1] offset:6144\n\t"
        "s_waitcnt lgkmcnt(12)\n\t"
        "v_mfma_f32_16x16x32_bf16 v[112:115], v[140:143], v[144:147], v[112:115]\n\t"
        "v_xor_b32 %[xa0], %[slot], %[xa0]\n\t"
        "v_xor_b32 %[xa1], %[slot], %[xa1]\n\t"
        "v_mfma_f32_16x16x32_bf16 v[116:119], v[140:143], v[148:151], v[116:119]\n\t"
        "v_xor_b32 %[wa0], %[slot], %[wa0]\n\t"
        "v_xor_b32 %[wa1], %[slot], %[wa1]\n\t"
        "v_mfma_f32_16x16x32_bf16 v[120:123], v[140:143], v[152:155], v[120:123]\n\t"
        "v_xor_b32 %[wa2], %[slot], %[wa2]\n\t"
        "v_xor_b32 %[wa3], %[slot], %[wa3]\n\t"
        "v_mfma_f32_16x16x32_bf16 v[124:127], v[140:143], v[156:159], v[124:127]\n\t"
        "s_mov_b32 %[slot], %[t0]\n\t"
        "s_cmp_eq_u32 %[cnt], 9\n\t"
        "s_cbranch_scc1 5f\n\t"
        "s_add_u32 %[cnt], %[cnt], 1\n\t"
        "s_cmp_eq_u32 %[cnt], %[dr]\n\t"
        "s_cbranch_scc1 6f\n\t"
        "4:\n\t"
        "s_cmp_eq_u32 %[cnt], 9\n\t"
        "s_waitcnt lgkmcnt(0)\n\t"
        "s_cbranch_scc1 9f\n\t"
        "s_barrier\n\t"
        "9:\n\t"
        "v_mfma_f32_16x16x32_bf16 v[64:67], v[160:163], v[176:179], v[64:67]\n\t"
        "ds_read_b128 v[128:131], %[xa0] offset:0\n\t"
        "v_mfma_f32_16x16x32_bf16 v[68:71], v[160:163], v[180:183], v[68:71]\n\t"
        "ds_read_b64_tr_b16 v[144:145], %[wa0] offset:16384\n\t"
        "v_mfma_f32_16x16x32_bf16 v[72:75], v[160:163], v[184:187], v[72:75]\n\t"
        "ds_read_b64_tr_b16 v[146:147], %[wa0] offset:17408\n\t"
        "v_mfma_f32_16x16x32_bf16 v[76:79], v[160:163], v[188:191], v[76:79]\n\t"
        "ds_read_b64_tr_b16 v[148:149], %[wa1] offset:16384\n\t"
        "v_mfma_f32_16x16x32_bf16 v[80:83], v[164:167], v[176:179], v[80:83]\n\t"
        "ds_read_b64_tr_b16 v[150:151], %[wa1] offset:17408\n\t"
        "v_mfma_f32_16x16x32_bf16 v[84:87], v[164:167], v[180:183], v[84:87]\n\t"
        "ds_read_b64_tr_b16 v[152:153], %[wa2] offset:16384\n\t"
        "v_mfma_f32_16x16x32_bf16 v[88:91], v[164:167], v[184:187], v[88:91]\n\t"
        "ds_read_b64_tr_b16 v[154:155], %[wa2] offset:17408\n\t"
        "v_mfma_f32_16x16x32_bf16 v[92:95], v[164:167], v[188:191], v[92:95]\n\t"
        "ds_read_b64_tr_b16 v[156:157], %[wa3] offset:16384\n\t"
        "v_mfma_f32_16x16x32_bf16 v[96:99], v[168:171], v[176:179], v[96:99]\n\t"
        "ds_read_b64_tr_b16 v[158:159], %[wa3] offset:17408\n\t"
        "v_mfma_f32_16x16x32_bf16 v[100:103], v[168:171], v[180:183], v[100:103]\n\t"
        "ds_read_b128 v[132:135], %[xa0] offset:2048\n\t"
        "v_mfma_f32_16x16x32_bf16 v[104:107], v[168:171], v[184:187], v[104:107]\n\t"
        "ds_read_b128 v[136:139], %[xa0] offset:4096\n\t"
        "v_mfma_f32_16x16x32_bf16 v[108:111], v[168:171], v[188:191], v[108:111]\n\t"
        "ds_read_b128 v[140:143], %[xa0] offset:6144\n\t"
        "v_mfma_f32_16x16x32_bf16 v[112:115], v[172:175], v[176:179], v[112:115]\n\t"
        "v_mfma_f32_16x16x32_bf16 v[116:119], v[172:175], v[180:183], v[116:119]\n\t"
        "v_mfma_f32_16x16x32_bf16 v[120:123], v[172:175], v[184:187], v[120:123]\n\t"
        "v_mfma_f32_16x16x32_bf16 v[124:127], v[172:175], v[188:191], v[124:127]\n\t"
        "s_branch 3b\n\t"
        "6:\n\t"
        "s_waitcnt vmcnt(0)\n\t"
        "s_branch 4b\n\t"
        "7:\n\t"
        "v_mov_b32 v192, 0x3f803f80\n\t"
        "v_mov_b32 v193, 0x3f803f80\n\t"
        "v_mov_b32 v194, 0x3f803f80\n\t"
        "v_mov_b32 v195, 0x3f803f80\n\t"
        "buffer_store_dwordx4 v[192:195], %[on], %[srd0], 0 offen sc1\n\t"
        "s_branch 8b\n\t"
        "5:\n\t"
        "s_waitcnt lgkmcnt(0)\n\t"
        "v_mfma_f32_16x16x32_bf16 v[64:67], v[160:163], v[176:179], v[64:67]\n\t"
        "v_mfma_f32_16x16x32_bf16 v[68:71], v[160:163], v[180:183], v[68:71]\n\t"
        "v_mfma_f32_16x16x32_bf16 v[72:75], v[160:163], v[184:187], v[72:75]\n\t"
        "v_mfma_f32_16x16x32_bf16 v[76:79], v[160:163], v[188:191], v[76:79]\n\t"
        "v_mfma_f32_16x16x32_bf16 v[80:83], v[164:167], v[176:179], v[80:83]\n\t"
        "v_mfma_f32_16x16x32_bf16 v[84:87], v[164:167], v[180:183], v[84:87]\n\t"
        "v_mfma_f32_16x16x32_bf16 v[88:91], v[164:167], v[184:187], v[88:91]\n\t"
        "v_mfma_f32_16x16x32_bf16 v[92:95], v[164:167], v[188:191], v[92:95]\n\t"
        "v_mfma_f32_16x16x32_bf16 v[96:99], v[168:171], v[176:179], v[96:99]\n\t"
        "v_mfma_f32_16x16x32_bf16 v[100:103], v[168:171], v[180:183], v[100:103]\n\t"
        "v_mfma_f32_16x16x32_bf16 v[104:107], v[168:171], v[184:187], v[104:107]\n\t"
        "v_mfma_f32_16x16x32_bf16 v[108:111], v[168:171], v[188:191], v[108:111]\n\t"
        "v_mfma_f32_16x16x32_bf16 v[112:115], v[172:175], v[176:179], v[112:115]\n\t"
        "v_mfma_f32_16x16x32_bf16 v[116:119], v[172:175], v[180:183], v[116:119]\n\t"
        "v_mfma_f32_16x16x32_bf16 v[120:123], v[172:175], v[184:187], v[120:123]\n\t"
        "v_mfma_f32_16x16x32_bf16 v[124:127], v[172:175], v[188:191], v[124:127]\n\t"
        "s_nop 7\n\t"
        GEMM_OPERANDS(ONES_OPERANDS));
}
DI u32x4 mksrd(const char* p, unsigned bytes) { const uint64_t b = (uint64_t)p; return (u32x4){rfl((unsigned)b), rfl((unsigned)(b >> 32)) & 0xffffu, bytes, 0x00020000u}; }
DI u32x4 swap16(unsigned a0, unsigned a1, unsigned b0, unsigned b1) {
    const auto x = __builtin_amdgcn_permlane16_swap(a0, b0, false, false), y = __builtin_amdgcn_permlane16_swap(a1, b1, false, false);
    return (u32x4){x[0], y[0], x[1], y[1]};
}

DI float swap_max(float x) { auto r = __builtin_amdgcn_permlane32_swap(__float_as_uint(x), __float_as_uint(x), false, false); return fmaxf(__uint_as_float(r[0]), __uint_as_float(r[1])); }
DI float swap_add(float x) { auto r = __builtin_amdgcn_permlane32_swap(__float_as_uint(x), __float_as_uint(x), false, false); return __uint_as_float(r[0]) + __uint_as_float(r[1]); }
DI float upper(float x) { auto r = __builtin_amdgcn_permlane32_swap(__float_as_uint(x), __float_as_uint(x), false, false); return __uint_as_float(r[1]); }
DI void feature_map(const f32x16& p, unsigned (&o)[10]) {
    constexpr float L2E = 1.4426950408889634f;
    float mx = p[0], mn = p[0];
#pragma unroll
    for (int i = 1; i < 10; i++) { mx = fmaxf(mx, p[i]); mn = fminf(mn, p[i]); }
    mx = swap_max(mx); mn = -swap_max(-mn);
    const f32x2 cp = {L2E, L2E}, cm = {-L2E, -L2E}, bx = {-mx * L2E, -mx * L2E}, bn = {mn * L2E, mn * L2E};
    f32x2 a[5], b[5];
#pragma unroll
    for (int i = 0; i < 5; i++) { const f32x2 v = {p[2 * i], p[2 * i + 1]}, x = __builtin_elementwise_fma(v, cp, bx), y = __builtin_elementwise_fma(v, cm, bn);
        a[i] = (f32x2){__builtin_amdgcn_exp2f(x[0]), __builtin_amdgcn_exp2f(x[1])}; b[i] = (f32x2){__builtin_amdgcn_exp2f(y[0]), __builtin_amdgcn_exp2f(y[1])}; }
    const f32x2 sa = (a[0] + a[1]) + (a[2] + a[3]) + a[4], sb = (b[0] + b[1]) + (b[2] + b[3]) + b[4];
    const float ra = __builtin_amdgcn_rcpf(swap_add(sa[0] + sa[1])), rb = __builtin_amdgcn_rcpf(swap_add(sb[0] + sb[1]));
#pragma unroll
    for (int i = 0; i < 5; i++) { const f32x2 x = a[i] * ra, y = b[i] * rb; o[i] = pk2(x[0], x[1]); o[5 + i] = pk2(y[0], y[1]); }
}
DI int vdim(int rho) { const int g = rho >> 3, hh = (rho >> 2) & 1, i = rho & 3; return g < 2 ? (hh ? 12 - 4 * g + i : 4 * g + i) : (g == 2 && !hh ? 16 + i : 20); }
DI unsigned vofs(int head, int d, int h) { const unsigned c = 20 * head + (d < 20 ? d : 0); return (c >> 7) * V_C + (d < 20 ? (c & 127) * 16 : 2048) + h * (2 * V_G); }
DI void store_o(__amdgpu_buffer_rsrc_t ro, unsigned so, unsigned sb, const f32x16& o, float inv) {
    __builtin_amdgcn_raw_buffer_store_b128(pk8(o, 0, inv), ro, so, 0, SC1);
    const unsigned c0 = pk2(o[8] * inv, o[9] * inv), c1 = pk2(o[10] * inv, o[11] * inv);
    __builtin_amdgcn_raw_buffer_store_b128((u32x4){c0, c1, (unsigned)__builtin_amdgcn_update_dpp(0u, c0, 0xF5, 0xf, 0xf, true), (unsigned)__builtin_amdgcn_update_dpp(0u, c1, 0xF5, 0xf, 0xf, true)}, ro, sb, 0, SC1);
}

constexpr int L_PHQ = 0;
constexpr int L_PE = 102400;
constexpr int L_PL = L_PE + 24576;
constexpr int L_KVW = L_PL + 24576;
constexpr int L_CTL = L_KVW + 3 * 3360;
constexpr int L_WPHI = L_CTL + 128;
constexpr int L_JUNK = L_WPHI + 1024;
constexpr int L_MAIN = L_JUNK + 256;
enum {   C_BOK = 2, C_ADRAIN = 3, C_KVW = 4, C_KVF = 5, C_PHQ = 6, C_OA = 7, C_OB = 8, C_AOK = 9, C_KVOK = 10, C_OOK = 11, C_W = 12, C_PE = 13  , C_PL = 15   };
struct AttLane { unsigned qa, qb0, qb1, ka, kb, vo; };
#define ATT_IN [qa] "v"(A.qa), [qb0] "v"(A.qb0), [qb1] "v"(A.qb1), [ka] "v"(A.ka), [kb] "v"(A.kb), [vo] "v"(A.vo), [sq0] "s"(sq0), [sq1] "s"(sq1), [srd] "s"(srd)
DI void attend(char* ws, unsigned sk, unsigned sv, unsigned sq0, unsigned sq1, const AttLane& A, f32x16 (&o)[4]) {
    const u32x4 srd = mksrd(ws + WS_BR, WS_BR);
    unsigned cnt = 3;
    asm volatile(
        "buffer_load_dwordx4 v[128:131], %[ka], %[srd], %[sk] offen offset:0 sc1\n\t"
        "buffer_load_dwordx2 v[144:145], %[kb], %[srd], %[sk] offen offset:0 sc1\n\t"
        "buffer_load_dwordx4 v[96:99], %[qa], %[srd], %[sq0] offen offset:0 sc1\n\t"
        "buffer_load_dwordx2 v[112:113], %[qb0], %[srd], %[sq0] offen offset:0 sc1\n\t"
        "buffer_load_dwordx2 v[120:121], %[qb1], %[srd], %[sq0] offen offset:0 sc1\n\t"
        "buffer_load_dwordx4 v[100:103], %[qa], %[srd], %[sq0] offen offset:512 sc1\n\t"
        "buffer_load_dwordx2 v[114:115], %[qb0], %[srd], %[sq0] offen offset:512 sc1\n\t"
        "buffer_load_dwordx2 v[122:123], %[qb1], %[srd], %[sq0] offen offset:512 sc1\n\t"
        "buffer_load_dwordx4 v[148:151], %[vo], %[srd], %[sv] offen offset:0 sc1\n\t"
        "buffer_load_dwordx4 v[152:155], %[vo], %[srd], %[sv] offen offset:2176 sc1\n\t"
        "s_add_u32 %[sv], %[sv], 8704\n\t"
        "buffer_load_dwordx4 v[104:107], %[qa], %[srd], %[sq1] offen offset:0 sc1\n\t"
        "buffer_load_dwordx2 v[116:117], %[qb0], %[srd], %[sq1] offen offset:0 sc1\n\t"
        "buffer_load_dwordx2 v[124:125], %[qb1], %[srd], %[sq1] offen offset:0 sc1\n\t"
        "buffer_load_dwordx4 v[108:111], %[qa], %[srd], %[sq1] offen offset:512 sc1\n\t"
        "buffer_load_dwordx2 v[118:119], %[qb0], %[srd], %[sq1] offen offset:512 sc1\n\t"
        "buffer_load_dwordx2 v[126:127], %[qb1], %[srd], %[sq1] offen offset:512 sc1\n\t"
        "buffer_load_dwordx4 v[132:135], %[ka], %[srd], %[sk] offen offset:512 sc1\n\t"
        "buffer_load_dwordx4 v[156:159], %[vo], %[srd], %[sv] offen offset:0 sc1\n\t"
        "buffer_load_dwordx4 v[160:163], %[vo], %[srd], %[sv] offen offset:2176 sc1\n\t"
        "s_add_u32 %[sv], %[sv], 8704\n\t"
        "buffer_load_dwordx4 v[136:139], %[ka], %[srd], %[sk] offen offset:1024 sc1\n\t"
        "s_waitcnt vmcnt(15)\n\t"
        "v_mfma_f32_32x32x8_bf16 v[64:79], v[144:145], v[112:113], 0\n\t"
        "v_mfma_f32_32x32x16_bf16 v[64:79], v[128:131], v[96:99], v[64:79]\n\t"
        "s_nop 11\n\t"
        "v_exp_f32 v64, v64\n\t"
        "v_exp_f32 v65, v65\n\t"
        "v_exp_f32 v66, v66\n\t"
        "v_exp_f32 v67, v67\n\t"
        "v_cvt_pk_bf16_f32 v64, v64, v65\n\t"
        "v_exp_f32 v68, v68\n\t"
        "s_waitcnt vmcnt(12)\n\t"
        "v_cvt_pk_bf16_f32 v65, v66, v67\n\t"
        "v_mfma_f32_32x32x8_bf16 v[80:95], v[144:145], v[114:115], 0\n\t"
        "v_exp_f32 v69, v69\n\t"
        "v_exp_f32 v70, v70\n\t"
        "v_exp_f32 v71, v71\n\t"
        "v_cvt_pk_bf16_f32 v66, v68, v69\n\t"
        "v_exp_f32 v72, v72\n\t"
        "v_cvt_pk_bf16_f32 v67, v70, v71\n\t"
        "v_mfma_f32_32x32x16_bf16 v[80:95], v[128:131], v[100:103], v[80:95]\n\t"
        "buffer_load_dwordx4 v[164:167], %[vo], %[srd], %[sv] offen offset:0 sc1\n\t"
        "v_exp_f32 v73, v73\n\t"
        "v_exp_f32 v74, v74\n\t"
        "buffer_load_dwordx4 v[168:171], %[vo], %[srd], %[sv] offen offset:2176 sc1\n\t"
        "v_exp_f32 v75, v75\n\t"
        "v_cvt_pk_bf16_f32 v72, v72, v73\n\t"
        "v_exp_f32 v76, v76\n\t"
        "s_add_u32 %[sv], %[sv], 8704\n\t"
        "v_cvt_pk_bf16_f32 v73, v74, v75\n\t"
        "s_waitcnt vmcnt(12)\n\t"
        "v_mfma_f32_32x32x16_bf16 v[0:15], v[148:151], v[64:67], 0\n\t"
        "v_exp_f32 v77, v77\n\t"
        "v_exp_f32 v78, v78\n\t"
        "v_exp_f32 v79, v79\n\t"
        "v_cvt_pk_bf16_f32 v74, v76, v77\n\t"
        "v_cvt_pk_bf16_f32 v75, v78, v79\n\t"
        "v_exp_f32 v80, v80\n\t"
        "v_exp_f32 v81, v81\n\t"
        "v_mfma_f32_32x32x16_bf16 v[0:15], v[152:155], v[72:75], v[0:15]\n\t"
        "v_exp_f32 v82, v82\n\t"
        "v_exp_f32 v83, v83\n\t"
        "v_cvt_pk_bf16_f32 v80, v80, v81\n\t"
        "v_exp_f32 v84, v84\n\t"
        "s_waitcnt vmcnt(9)\n\t"
        "v_cvt_pk_bf16_f32 v81, v82, v83\n\t"
        "v_mfma_f32_32x32x8_bf16 v[64:79], v[144:145], v[116:117], 0\n\t"
        "v_exp_f32 v85, v85\n\t"
        "v_exp_f32 v86, v86\n\t"
        "v_exp_f32 v87, v87\n\t"
        "v_cvt_pk_bf16_f32 v82, v84, v85\n\t"
        "v_exp_f32 v88, v88\n\t"
        "v_cvt_pk_bf16_f32 v83, v86, v87\n\t"
        "v_mfma_f32_32x32x16_bf16 v[64:79], v[128:131], v[104:107], v[64:79]\n\t"
        "buffer_load_dwordx2 v[146:147], %[kb], %[srd], %[sk] offen offset:1024 sc1\n\t"
        "v_exp_f32 v89, v89\n\t"
        "v_exp_f32 v90, v90\n\t"
        "v_exp_f32 v91, v91\n\t"
        "v_cvt_pk_bf16_f32 v88, v88, v89\n\t"
        "v_exp_f32 v92, v92\n\t"
        "v_cvt_pk_bf16_f32 v89, v90, v91\n\t"
        "v_mfma_f32_32x32x16_bf16 v[16:31], v[148:151], v[80:83], 0\n\t"
        "v_exp_f32 v93, v93\n\t"
        "v_exp_f32 v94, v94\n\t"
        "v_exp_f32 v95, v95\n\t"
        "v_cvt_pk_bf16_f32 v90, v92, v93\n\t"
        "v_cvt_pk_bf16_f32 v91, v94, v95\n\t"
        "v_exp_f32 v64, v64\n\t"
        "v_exp_f32 v65, v65\n\t"
        "v_mfma_f32_32x32x16_bf16 v[16:31], v[152:155], v[88:91], v[16:31]\n\t"
        "v_exp_f32 v66, v66\n\t"
        "v_exp_f32 v67, v67\n\t"
        "v_cvt_pk_bf16_f32 v64, v64, v65\n\t"
        "v_exp_f32 v68, v68\n\t"
        "s_waitcnt vmcnt(7)\n\t"
        "v_cvt_pk_bf16_f32 v65, v66, v67\n\t"
        "v_mfma_f32_32x32x8_bf16 v[80:95], v[144:145], v[118:119], 0\n\t"
        "v_exp_f32 v69, v69\n\t"
        "v_exp_f32 v70, v70\n\t"
        "v_exp_f32 v71, v71\n\t"
        "v_cvt_pk_bf16_f32 v66, v68, v69\n\t"
        "v_exp_f32 v72, v72\n\t"
        "v_cvt_pk_bf16_f32 v67, v70, v71\n\t"
        "v_mfma_f32_32x32x16_bf16 v[80:95], v[128:131], v[108:111], v[80:95]\n\t"
        "buffer_load_dwordx4 v[140:143], %[ka], %[srd], %[sk] offen offset:1536 sc1\n\t"
        "v_exp_f32 v73, v73\n\t"
        "v_exp_f32 v74, v74\n\t"
        "v_exp_f32 v75, v75\n\t"
        "v_cvt_pk_bf16_f32 v72, v72, v73\n\t"
        "v_exp_f32 v76, v76\n\t"
        "v_cvt_pk_bf16_f32 v73, v74, v75\n\t"
        "v_mfma_f32_32x32x16_bf16 v[32:47], v[148:151], v[64:67], 0\n\t"
        "v_exp_f32 v77, v77\n\t"
        "v_exp_f32 v78, v78\n\t"
        "v_exp_f32 v79, v79\n\t"
        "v_cvt_pk_bf16_f32 v74, v76, v77\n\t"
        "v_cvt_pk_bf16_f32 v75, v78, v79\n\t"
        "v_exp_f32 v80, v80\n\t"
        "v_exp_f32 v81, v81\n\t"
        "v_mfma_f32_32x32x16_bf16 v[32:47], v[152:155], v[72:75], v[32:47]\n\t"
        "v_exp_f32 v82, v82\n\t"
        "v_exp_f32 v83, v83\n\t"
        "v_cvt_pk_bf16_f32 v80, v80, v81\n\t"
        "v_exp_f32 v84, v84\n\t"
        "s_waitcnt vmcnt(5)\n\t"
        "v_cvt_pk_bf16_f32 v81, v82, v83\n\t"
        "v_mfma_f32_32x32x8_bf16 v[64:79], v[144:145], v[120:121], 0\n\t"
        "v_exp_f32 v85, v85\n\t"
        "v_exp_f32 v86, v86\n\t"
        "v_exp_f32 v87, v87\n\t"
        "v_cvt_pk_bf16_f32 v82, v84, v85\n\t"
        "v_exp_f32 v88, v88\n\t"
        "v_cvt_pk_bf16_f32 v83, v86, v87\n\t"
        "v_mfma_f32_32x32x16_bf16 v[64:79], v[132:135], v[96:99], v[64:79]\n\t"
        "v_exp_f32 v89, v89\n\t"
        "v_exp_f32 v90, v90\n\t"
        "v_exp_f32 v91, v91\n\t"
        "v_cvt_pk_bf16_f32 v88, v88, v89\n\t"
        "v_exp_f32 v92, v92\n\t"
        "v_cvt_pk_bf16_f32 v89, v90, v91\n\t"
        "v_mfma_f32_32x32x16_bf16 v[48:63], v[148:151], v[80:83], 0\n\t"
        "v_exp_f32 v93, v93\n\t"
        "v_exp_f32 v94, v94\n\t"
        "v_exp_f32 v95, v95\n\t"
        "v_cvt_pk_bf16_f32 v90, v92, v93\n\t"
        "v_cvt_pk_bf16_f32 v91, v94, v95\n\t"
        "s_branch 5f\n\t"
        "1:\n\t"
        "v_exp_f32 v64, v64\n\t"
        "v_exp_f32 v65, v65\n\t"
        "v_mfma_f32_32x32x16_bf16 v[48:63], v[176:179], v[88:91], v[48:63]\n\t"
        "v_exp_f32 v66, v66\n\t"
        "v_exp_f32 v67, v67\n\t"
        "v_cvt_pk_bf16_f32 v64, v64, v65\n\t"
        "v_exp_f32 v68, v68\n\t"
        "v_cvt_pk_bf16_f32 v65, v66, v67\n\t"
        "v_mfma_f32_32x32x8_bf16 v[80:95], v[144:145], v[114:115], 0\n\t"
        "v_exp_f32 v69, v69\n\t"
        "v_exp_f32 v70, v70\n\t"
        "v_exp_f32 v71, v71\n\t"
        "v_cvt_pk_bf16_f32 v66, v68, v69\n\t"
        "v_exp_f32 v72, v72\n\t"
        "v_cvt_pk_bf16_f32 v67, v70, v71\n\t"
        "v_mfma_f32_32x32x16_bf16 v[80:95], v[128:131], v[100:103], v[80:95]\n\t"
        "buffer_load_dwordx4 v[164:167], %[vo], %[srd], %[sv] offen offset:0 sc1\n\t"
        "v_exp_f32 v73, v73\n\t"
        "v_exp_f32 v74, v74\n\t"
        "buffer_load_dwordx4 v[168:171], %[vo], %[srd], %[sv] offen offset:2176 sc1\n\t"
        "v_exp_f32 v75, v75\n\t"
        "v_cvt_pk_bf16_f32 v72, v72, v73\n\t"
        "v_exp_f32 v76, v76\n\t"
        "s_add_u32 %[sv], %[sv], 8704\n\t"
        "v_cvt_pk_bf16_f32 v73, v74, v75\n\t"
        "v_mfma_f32_32x32x16_bf16 v[0:15], v[148:151], v[64:67], v[0:15]\n\t"
        "v_exp_f32 v77, v77\n\t"
        "v_exp_f32 v78, v78\n\t"
        "v_exp_f32 v79, v79\n\t"
        "v_cvt_pk_bf16_f32 v74, v76, v77\n\t"
        "v_cvt_pk_bf16_f32 v75, v78, v79\n\t"
        "v_exp_f32 v80, v80\n\t"
        "v_exp_f32 v81, v81\n\t"
        "v_mfma_f32_32x32x16_bf16 v[0:15], v[152:155], v[72:75], v[0:15]\n\t"
        "v_exp_f32 v82, v82\n\t"
        "v_exp_f32 v83, v83\n\t"
        "v_cvt_pk_bf16_f32 v80, v80, v81\n\t"
        "v_exp_f32 v84, v84\n\t"
        "v_cvt_pk_bf16_f32 v81, v82, v83\n\t"
        "v_mfma_f32_32x32x8_bf16 v[64:79], v[144:145], v[116:117], 0\n\t"
        "v_exp_f32 v85, v85\n\t"
        "v_exp_f32 v86, v86\n\t"
        "v_exp_f32 v87, v87\n\t"
        "v_cvt_pk_bf16_f32 v82, v84, v85\n\t"
        "v_exp_f32 v88, v88\n\t"
        "v_cvt_pk_bf16_f32 v83, v86, v87\n\t"
        "v_mfma_f32_32x32x16_bf16 v[64:79], v[128:131], v[104:107], v[64:79]\n\t"
        "buffer_load_dwordx2 v[146:147], %[kb], %[srd], %[sk] offen offset:1024 sc1\n\t"
        "v_exp_f32 v89, v89\n\t"
        "v_exp_f32 v90, v90\n\t"
        "v_exp_f32 v91, v91\n\t"
        "v_cvt_pk_bf16_f32 v88, v88, v89\n\t"
        "v_exp_f32 v92, v92\n\t"
        "v_cvt_pk_bf16_f32 v89, v90, v91\n\t"
        "v_mfma_f32_32x32x16_bf16 v[16:31], v[148:151], v[80:83], v[16:31]\n\t"
        "v_exp_f32 v93, v93\n\t"
        "v_exp_f32 v94, v94\n\t"
        "v_exp_f32 v95, v95\n\t"
        "v_cvt_pk_bf16_f32 v90, v92, v93\n\t"
        "v_cvt_pk_bf16_f32 v91, v94, v95\n\t"
        "v_exp_f32 v64, v64\n\t"
        "v_exp_f32 v65, v65\n\t"
        "v_mfma_f32_32x32x16_bf16 v[16:31], v[152:155], v[88:91], v[16:31]\n\t"
        "v_exp_f32 v66, v66\n\t"
        "v_exp_f32 v67, v67\n\t"
        "v_cvt_pk_bf16_f32 v64, v64, v65\n\t"
        "v_exp_f32 v68, v68\n\t"
        "v_cvt_pk_bf16_f32 v65, v66, v67\n\t"
        "v_mfma_f32_32x32x8_bf16 v[80:95], v[144:145], v[118:119], 0\n\t"
        "v_exp_f32 v69, v69\n\t"
        "v_exp_f32 v70, v70\n\t"
        "v_exp_f32 v71, v71\n\t"
        "v_cvt_pk_bf16_f32 v66, v68, v69\n\t"
        "v_exp_f32 v72, v72\n\t"
        "v_cvt_pk_bf16_f32 v67, v70, v71\n\t"
        "v_mfma_f32_32x32x16_bf16 v[80:95], v[128:131], v[108:111], v[80:95]\n\t"
        "buffer_load_dwordx4 v[140:143], %[ka], %[srd], %[sk] offen offset:1536 sc1\n\t"
        "v_exp_f32 v73, v73\n\t"
        "v_exp_f32 v74, v74\n\t"
        "v_exp_f32 v75, v75\n\t"
        "v_cvt_pk_bf16_f32 v72, v72, v73\n\t"
        "v_exp_f32 v76, v76\n\t"
        "v_cvt_pk_bf16_f32 v73, v74, v75\n\t"
        "v_mfma_f32_32x32x16_bf16 v[32:47], v[148:151], v[64:67], v[32:47]\n\t"
        "v_exp_f32 v77, v77\n\t"
        "v_exp_f32 v78, v78\n\t"
        "v_exp_f32 v79, v79\n\t"
        "v_cvt_pk_bf16_f32 v74, v76, v77\n\t"
        "v_cvt_pk_bf16_f32 v75, v78, v79\n\t"
        "v_exp_f32 v80, v80\n\t"
        "v_exp_f32 v81, v81\n\t"
        "v_mfma_f32_32x32x16_bf16 v[32:47], v[152:155], v[72:75], v[32:47]\n\t"
        "v_exp_f32 v82, v82\n\t"
        "v_exp_f32 v83, v83\n\t"
        "v_cvt_pk_bf16_f32 v80, v80, v81\n\t"
        "v_exp_f32 v84, v84\n\t"
        "s_waitcnt vmcnt(5)\n\t"
        "v_cvt_pk_bf16_f32 v81, v82, v83\n\t"
        "v_mfma_f32_32x32x8_bf16 v[64:79], v[144:145], v[120:121], 0\n\t"
        "v_exp_f32 v85, v85\n\t"
        "v_exp_f32 v86, v86\n\t"
        "v_exp_f32 v87, v87\n\t"
        "v_cvt_pk_bf16_f32 v82, v84, v85\n\t"
        "v_exp_f32 v88, v88\n\t"
        "v_cvt_pk_bf16_f32 v83, v86, v87\n\t"
        "v_mfma_f32_32x32x16_bf16 v[64:79], v[132:135], v[96:99], v[64:79]\n\t"
        "v_exp_f32 v89, v89\n\t"
        "v_exp_f32 v90, v90\n\t"
        "v_exp_f32 v91, v91\n\t"
        "v_cvt_pk_bf16_f32 v88, v88, v89\n\t"
        "v_exp_f32 v92, v92\n\t"
        "v_cvt_pk_bf16_f32 v89, v90, v91\n\t"
        "v_mfma_f32_32x32x16_bf16 v[48:63], v[148:151], v[80:83], v[48:63]\n\t"
        "v_exp_f32 v93, v93\n\t"
        "v_exp_f32 v94, v94\n\t"
        "v_exp_f32 v95, v95\n\t"
        "v_cvt_pk_bf16_f32 v90, v92, v93\n\t"
        "v_cvt_pk_bf16_f32 v91, v94, v95\n\t"
        "5:\n\t"
        "v_exp_f32 v64, v64\n\t"
        "v_exp_f32 v65, v65\n\t"
        "v_mfma_f32_32x32x16_bf16 v[48:63], v[152:155], v[88:91], v[48:63]\n\t"
        "v_exp_f32 v66, v66\n\t"
        "v_exp_f32 v67, v67\n\t"
        "v_cvt_pk_bf16_f32 v64, v64, v65\n\t"
        "v_exp_f32 v68, v68\n\t"
        "v_cvt_pk_bf16_f32 v65, v66, v67\n\t"
        "v_mfma_f32_32x32x8_bf16 v[80:95], v[144:145], v[122:123], 0\n\t"
        "v_exp_f32 v69, v69\n\t"
        "v_exp_f32 v70, v70\n\t"
        "v_exp_f32 v71, v71\n\t"
        "v_cvt_pk_bf16_f32 v66, v68, v69\n\t"
        "v_exp_f32 v72, v72\n\t"
        "v_cvt_pk_bf16_f32 v67, v70, v71\n\t"
        "v_mfma_f32_32x32x16_bf16 v[80:95], v[132:135], v[100:103], v[80:95]\n\t"
        "buffer_load_dwordx4 v[172:175], %[vo], %[srd], %[sv] offen offset:0 sc1\n\t"
        "v_exp_f32 v73, v73\n\t"
        "v_exp_f32 v74, v74\n\t"
        "buffer_load_dwordx4 v[176:179], %[vo], %[srd], %[sv] offen offset:2176 sc1\n\t"
        "v_exp_f32 v75, v75\n\t"
        "v_cvt_pk_bf16_f32 v72, v72, v73\n\t"
        "v_exp_f32 v76, v76\n\t"
        "s_add_u32 %[sv], %[sv], 8704\n\t"
        "v_cvt_pk_bf16_f32 v73, v74, v75\n\t"
        "v_mfma_f32_32x32x16_bf16 v[0:15], v[156:159], v[64:67], v[0:15]\n\t"
        "v_exp_f32 v77, v77\n\t"
        "v_exp_f32 v78, v78\n\t"
        "v_exp_f32 v79, v79\n\t"
        "v_cvt_pk_bf16_f32 v74, v76, v77\n\t"
        "v_cvt_pk_bf16_f32 v75, v78, v79\n\t"
        "v_exp_f32 v80, v80\n\t"
        "v_exp_f32 v81, v81\n\t"
        "v_mfma_f32_32x32x16_bf16 v[0:15], v[160:163], v[72:75], v[0:15]\n\t"
        "v_exp_f32 v82, v82\n\t"
        "v_exp_f32 v83, v83\n\t"
        "v_cvt_pk_bf16_f32 v80, v80, v81\n\t"
        "v_exp_f32 v84, v84\n\t"
        "v_cvt_pk_bf16_f32 v81, v82, v83\n\t"
        "v_mfma_f32_32x32x8_bf16 v[64:79], v[144:145], v[124:125], 0\n\t"
        "v_exp_f32 v85, v85\n\t"
        "v_exp_f32 v86, v86\n\t"
        "v_exp_f32 v87, v87\n\t"
        "v_cvt_pk_bf16_f32 v82, v84, v85\n\t"
        "v_exp_f32 v88, v88\n\t"
        "v_cvt_pk_bf16_f32 v83, v86, v87\n\t"
        "v_mfma_f32_32x32x16_bf16 v[64:79], v[132:135], v[104:107], v[64:79]\n\t"
        "v_exp_f32 v89, v89\n\t"
        "v_exp_f32 v90, v90\n\t"
        "v_exp_f32 v91, v91\n\t"
        "v_cvt_pk_bf16_f32 v88, v88, v89\n\t"
        "v_exp_f32 v92, v92\n\t"
        "v_cvt_pk_bf16_f32 v89, v90, v91\n\t"
        "v_mfma_f32_32x32x16_bf16 v[16:31], v[156:159], v[80:83], v[16:31]\n\t"
        "v_exp_f32 v93, v93\n\t"
        "v_exp_f32 v94, v94\n\t"
        "v_exp_f32 v95, v95\n\t"
        "v_cvt_pk_bf16_f32 v90, v92, v93\n\t"
        "v_cvt_pk_bf16_f32 v91, v94, v95\n\t"
        "v_exp_f32 v64, v64\n\t"
        "v_exp_f32 v65, v65\n\t"
        "v_mfma_f32_32x32x16_bf16 v[16:31], v[160:163], v[88:91], v[16:31]\n\t"
        "v_exp_f32 v66, v66\n\t"
        "v_exp_f32 v67, v67\n\t"
        "v_cvt_pk_bf16_f32 v64, v64, v65\n\t"
        "v_exp_f32 v68, v68\n\t"
        "v_cvt_pk_bf16_f32 v65, v66, v67\n\t"
        "v_mfma_f32_32x32x8_bf16 v[80:95], v[144:145], v[126:127], 0\n\t"
        "v_exp_f32 v69, v69\n\t"
        "v_exp_f32 v70, v70\n\t"
        "v_exp_f32 v71, v71\n\t"
        "v_cvt_pk_bf16_f32 v66, v68, v69\n\t"
        "v_exp_f32 v72, v72\n\t"
        "v_cvt_pk_bf16_f32 v67, v70, v71\n\t"
        "v_mfma_f32_32x32x16_bf16 v[80:95], v[132:135], v[108:111], v[80:95]\n\t"
        "buffer_load_dwordx4 v[128:131], %[ka], %[srd], %[sk] offen offset:2048 sc1\n\t"
        "v_exp_f32 v73, v73\n\t"
        "v_exp_f32 v74, v74\n\t"
        "v_exp_f32 v75, v75\n\t"
        "v_cvt_pk_bf16_f32 v72, v72, v73\n\t"
        "v_exp_f32 v76, v76\n\t"
        "v_cvt_pk_bf16_f32 v73, v74, v75\n\t"
        "v_mfma_f32_32x32x16_bf16 v[32:47], v[156:159], v[64:67], v[32:47]\n\t"
        "v_exp_f32 v77, v77\n\t"
        "v_exp_f32 v78, v78\n\t"
        "v_exp_f32 v79, v79\n\t"
        "v_cvt_pk_bf16_f32 v74, v76, v77\n\t"
        "v_cvt_pk_bf16_f32 v75, v78, v79\n\t"
        "v_exp_f32 v80, v80\n\t"
        "v_exp_f32 v81, v81\n\t"
        "v_mfma_f32_32x32x16_bf16 v[32:47], v[160:163], v[72:75], v[32:47]\n\t"
        "v_exp_f32 v82, v82\n\t"
        "v_exp_f32 v83, v83\n\t"
        "v_cvt_pk_bf16_f32 v80, v80, v81\n\t"
        "v_exp_f32 v84, v84\n\t"
        "s_waitcnt vmcnt(4)\n\t"
        "v_cvt_pk_bf16_f32 v81, v82, v83\n\t"
        "v_mfma_f32_32x32x8_bf16 v[64:79], v[146:147], v[112:113], 0\n\t"
        "v_exp_f32 v85, v85\n\t"
        "v_exp_f32 v86, v86\n\t"
        "v_exp_f32 v87, v87\n\t"
        "v_cvt_pk_bf16_f32 v82, v84, v85\n\t"
        "v_exp_f32 v88, v88\n\t"
        "v_cvt_pk_bf16_f32 v83, v86, v87\n\t"
        "v_mfma_f32_32x32x16_bf16 v[64:79], v[136:139], v[96:99], v[64:79]\n\t"
        "v_exp_f32 v89, v89\n\t"
        "v_exp_f32 v90, v90\n\t"
        "v_exp_f32 v91, v91\n\t"
        "v_cvt_pk_bf16_f32 v88, v88, v89\n\t"
        "v_exp_f32 v92, v92\n\t"
        "v_cvt_pk_bf16_f32 v89, v90, v91\n\t"
        "v_mfma_f32_32x32x16_bf16 v[48:63], v[156:159], v[80:83], v[48:63]\n\t"
        "v_exp_f32 v93, v93\n\t"
        "v_exp_f32 v94, v94\n\t"
        "v_exp_f32 v95, v95\n\t"
        "v_cvt_pk_bf16_f32 v90, v92, v93\n\t"
        "v_cvt_pk_bf16_f32 v91, v94, v95\n\t"
        "v_exp_f32 v64, v64\n\t"
        "v_exp_f32 v65, v65\n\t"
        "v_mfma_f32_32x32x16_bf16 v[48:63], v[160:163], v[88:91], v[48:63]\n\t"
        "v_exp_f32 v66, v66\n\t"
        "v_exp_f32 v67, v67\n\t"
        "v_cvt_pk_bf16_f32 v64, v64, v65\n\t"
        "v_exp_f32 v68, v68\n\t"
        "v_cvt_pk_bf16_f32 v65, v66, v67\n\t"
        "v_mfma_f32_32x32x8_bf16 v[80:95], v[146:147], v[114:115], 0\n\t"
        "v_exp_f32 v69, v69\n\t"
        "v_exp_f32 v70, v70\n\t"
        "v_exp_f32 v71, v71\n\t"
        "v_cvt_pk_bf16_f32 v66, v68, v69\n\t"
        "v_exp_f32 v72, v72\n\t"
        "v_cvt_pk_bf16_f32 v67, v70, v71\n\t"
        "v_mfma_f32_32x32x16_bf16 v[80:95], v[136:139], v[100:103], v[80:95]\n\t"
        "buffer_load_dwordx4 v[148:151], %[vo], %[srd], %[sv] offen offset:0 sc1\n\t"
        "v_exp_f32 v73, v73\n\t"
        "v_exp_f32 v74, v74\n\t"
        "buffer_load_dwordx4 v[152:155], %[vo], %[srd], %[sv] offen offset:2176 sc1\n\t"
        "v_exp_f32 v75, v75\n\t"
        "v_cvt_pk_bf16_f32 v72, v72, v73\n\t"
        "v_exp_f32 v76, v76\n\t"
        "s_add_u32 %[sv], %[sv], 8704\n\t"
        "v_cvt_pk_bf16_f32 v73, v74, v75\n\t"
        "v_mfma_f32_32x32x16_bf16 v[0:15], v[164:167], v[64:67], v[0:15]\n\t"
        "v_exp_f32 v77, v77\n\t"
        "v_exp_f32 v78, v78\n\t"
        "v_exp_f32 v79, v79\n\t"
        "v_cvt_pk_bf16_f32 v74, v76, v77\n\t"
        "v_cvt_pk_bf16_f32 v75, v78, v79\n\t"
        "v_exp_f32 v80, v80\n\t"
        "v_exp_f32 v81, v81\n\t"
        "v_mfma_f32_32x32x16_bf16 v[0:15], v[168:171], v[72:75], v[0:15]\n\t"
        "v_exp_f32 v82, v82\n\t"
        "v_exp_f32 v83, v83\n\t"
        "v_cvt_pk_bf16_f32 v80, v80, v81\n\t"
        "v_exp_f32 v84, v84\n\t"
        "v_cvt_pk_bf16_f32 v81, v82, v83\n\t"
        "v_mfma_f32_32x32x8_bf16 v[64:79], v[146:147], v[116:117], 0\n\t"
        "v_exp_f32 v85, v85\n\t"
        "v_exp_f32 v86, v86\n\t"
        "v_exp_f32 v87, v87\n\t"
        "v_cvt_pk_bf16_f32 v82, v84, v85\n\t"
        "v_exp_f32 v88, v88\n\t"
        "v_cvt_pk_bf16_f32 v83, v86, v87\n\t"
        "v_mfma_f32_32x32x16_bf16 v[64:79], v[136:139], v[104:107], v[64:79]\n\t"
        "buffer_load_dwordx2 v[144:145], %[kb], %[srd], %[sk] offen offset:2048 sc1\n\t"
        "v_exp_f32 v89, v89\n\t"
        "v_exp_f32 v90, v90\n\t"
        "v_exp_f32 v91, v91\n\t"
        "v_cvt_pk_bf16_f32 v88, v88, v89\n\t"
        "v_exp_f32 v92, v92\n\t"
        "v_cvt_pk_bf16_f32 v89, v90, v91\n\t"
        "v_mfma_f32_32x32x16_bf16 v[16:31], v[164:167], v[80:83], v[16:31]\n\t"
        "v_exp_f32 v93, v93\n\t"
        "v_exp_f32 v94, v94\n\t"
        "v_exp_f32 v95, v95\n\t"
        "v_cvt_pk_bf16_f32 v90, v92, v93\n\t"
        "v_cvt_pk_bf16_f32 v91, v94, v95\n\t"
        "v_exp_f32 v64, v64\n\t"
        "v_exp_f32 v65, v65\n\t"
        "v_mfma_f32_32x32x16_bf16 v[16:31], v[168:171], v[88:91], v[16:31]\n\t"
        "v_exp_f32 v66, v66\n\t"
        "v_exp_f32 v67, v67\n\t"
        "v_cvt_pk_bf16_f32 v64, v64, v65\n\t"
        "v_exp_f32 v68, v68\n\t"
        "v_cvt_pk_bf16_f32 v65, v66, v67\n\t"
        "v_mfma_f32_32x32x8_bf16 v[80:95], v[146:147], v[118:119], 0\n\t"
        "v_exp_f32 v69, v69\n\t"
        "v_exp_f32 v70, v70\n\t"
        "v_exp_f32 v71, v71\n\t"
        "v_cvt_pk_bf16_f32 v66, v68, v69\n\t"
        "v_exp_f32 v72, v72\n\t"
        "v_cvt_pk_bf16_f32 v67, v70, v71\n\t"
        "v_mfma_f32_32x32x16_bf16 v[80:95], v[136:139], v[108:111], v[80:95]\n\t"
        "buffer_load_dwordx4 v[132:135], %[ka], %[srd], %[sk] offen offset:2560 sc1\n\t"
        "v_exp_f32 v73, v73\n\t"
        "v_exp_f32 v74, v74\n\t"
        "v_exp_f32 v75, v75\n\t"
        "v_cvt_pk_bf16_f32 v72, v72, v73\n\t"
        "v_exp_f32 v76, v76\n\t"
        "v_cvt_pk_bf16_f32 v73, v74, v75\n\t"
        "v_mfma_f32_32x32x16_bf16 v[32:47], v[164:167], v[64:67], v[32:47]\n\t"
        "v_exp_f32 v77, v77\n\t"
        "v_exp_f32 v78, v78\n\t"
        "v_exp_f32 v79, v79\n\t"
        "v_cvt_pk_bf16_f32 v74, v76, v77\n\t"
        "v_cvt_pk_bf16_f32 v75, v78, v79\n\t"
        "v_exp_f32 v80, v80\n\t"
        "v_exp_f32 v81, v81\n\t"
        "v_mfma_f32_32x32x16_bf16 v[32:47], v[168:171], v[72:75], v[32:47]\n\t"
        "v_exp_f32 v82, v82\n\t"
        "v_exp_f32 v83, v83\n\t"
        "v_cvt_pk_bf16_f32 v80, v80, v81\n\t"
        "v_exp_f32 v84, v84\n\t"
        "s_waitcnt vmcnt(5)\n\t"
        "v_cvt_pk_bf16_f32 v81, v82, v83\n\t"
        "v_mfma_f32_32x32x8_bf16 v[64:79], v[146:147], v[120:121], 0\n\t"
        "v_exp_f32 v85, v85\n\t"
        "v_exp_f32 v86, v86\n\t"
        "v_exp_f32 v87, v87\n\t"
        "v_cvt_pk_bf16_f32 v82, v84, v85\n\t"
        "v_exp_f32 v88, v88\n\t"
        "v_cvt_pk_bf16_f32 v83, v86, v87\n\t"
        "v_mfma_f32_32x32x16_bf16 v[64:79], v[140:143], v[96:99], v[64:79]\n\t"
        "v_exp_f32 v89, v89\n\t"
        "v_exp_f32 v90, v90\n\t"
        "v_exp_f32 v91, v91\n\t"
        "v_cvt_pk_bf16_f32 v88, v88, v89\n\t"
        "v_exp_f32 v92, v92\n\t"
        "v_cvt_pk_bf16_f32 v89, v90, v91\n\t"
        "v_mfma_f32_32x32x16_bf16 v[48:63], v[164:167], v[80:83], v[48:63]\n\t"
        "v_exp_f32 v93, v93\n\t"
        "v_exp_f32 v94, v94\n\t"
        "v_exp_f32 v95, v95\n\t"
        "v_cvt_pk_bf16_f32 v90, v92, v93\n\t"
        "v_cvt_pk_bf16_f32 v91, v94, v95\n\t"
        "v_exp_f32 v64, v64\n\t"
        "v_exp_f32 v65, v65\n\t"
        "v_mfma_f32_32x32x16_bf16 v[48:63], v[168:171], v[88:91], v[48:63]\n\t"
        "v_exp_f32 v66, v66\n\t"
        "v_exp_f32 v67, v67\n\t"
        "v_cvt_pk_bf16_f32 v64, v64, v65\n\t"
        "v_exp_f32 v68, v68\n\t"
        "v_cvt_pk_bf16_f32 v65, v66, v67\n\t"
        "v_mfma_f32_32x32x8_bf16 v[80:95], v[146:147], v[122:123], 0\n\t"
        "v_exp_f32 v69, v69\n\t"
        "v_exp_f32 v70, v70\n\t"
        "v_exp_f32 v71, v71\n\t"
        "v_cvt_pk_bf16_f32 v66, v68, v69\n\t"
        "v_exp_f32 v72, v72\n\t"
        "v_cvt_pk_bf16_f32 v67, v70, v71\n\t"
        "v_mfma_f32_32x32x16_bf16 v[80:95], v[140:143], v[100:103], v[80:95]\n\t"
        "buffer_load_dwordx4 v[156:159], %[vo], %[srd], %[sv] offen offset:0 sc1\n\t"
        "v_exp_f32 v73, v73\n\t"
        "v_exp_f32 v74, v74\n\t"
        "buffer_load_dwordx4 v[160:163], %[vo], %[srd], %[sv] offen offset:2176 sc1\n\t"
        "v_exp_f32 v75, v75\n\t"
        "v_cvt_pk_bf16_f32 v72, v72, v73\n\t"
        "v_exp_f32 v76, v76\n\t"
        "s_add_u32 %[sv], %[sv], 8704\n\t"
        "v_cvt_pk_bf16_f32 v73, v74, v75\n\t"
        "v_mfma_f32_32x32x16_bf16 v[0:15], v[172:175], v[64:67], v[0:15]\n\t"
        "v_exp_f32 v77, v77\n\t"
        "v_exp_f32 v78, v78\n\t"
        "v_exp_f32 v79, v79\n\t"
        "v_cvt_pk_bf16_f32 v74, v76, v77\n\t"
        "v_cvt_pk_bf16_f32 v75, v78, v79\n\t"
        "v_exp_f32 v80, v80\n\t"
        "v_exp_f32 v81, v81\n\t"
        "v_mfma_f32_32x32x16_bf16 v[0:15], v[176:179], v[72:75], v[0:15]\n\t"
        "v_exp_f32 v82, v82\n\t"
        "v_exp_f32 v83, v83\n\t"
        "v_cvt_pk_bf16_f32 v80, v80, v81\n\t"
        "v_exp_f32 v84, v84\n\t"
        "v_cvt_pk_bf16_f32 v81, v82, v83\n\t"
        "v_mfma_f32_32x32x8_bf16 v[64:79], v[146:147], v[124:125], 0\n\t"
        "v_exp_f32 v85, v85\n\t"
        "v_exp_f32 v86, v86\n\t"
        "v_exp_f32 v87, v87\n\t"
        "v_cvt_pk_bf16_f32 v82, v84, v85\n\t"
        "v_exp_f32 v88, v88\n\t"
        "v_cvt_pk_bf16_f32 v83, v86, v87\n\t"
        "v_mfma_f32_32x32x16_bf16 v[64:79], v[140:143], v[104:107], v[64:79]\n\t"
        "v_exp_f32 v89, v89\n\t"
        "v_exp_f32 v90, v90\n\t"
        "v_exp_f32 v91, v91\n\t"
        "v_cvt_pk_bf16_f32 v88, v88, v89\n\t"
        "v_exp_f32 v92, v92\n\t"
        "v_cvt_pk_bf16_f32 v89, v90, v91\n\t"
        "v_mfma_f32_32x32x16_bf16 v[16:31], v[172:175], v[80:83], v[16:31]\n\t"
        "v_exp_f32 v93, v93\n\t"
        "v_exp_f32 v94, v94\n\t"
        "v_exp_f32 v95, v95\n\t"
        "v_cvt_pk_bf16_f32 v90, v92, v93\n\t"
        "v_cvt_pk_bf16_f32 v91, v94, v95\n\t"
        "v_exp_f32 v64, v64\n\t"
        "v_exp_f32 v65, v65\n\t"
        "v_mfma_f32_32x32x16_bf16 v[16:31], v[176:179], v[88:91], v[16:31]\n\t"
        "v_exp_f32 v66, v66\n\t"
        "v_exp_f32 v67, v67\n\t"
        "v_cvt_pk_bf16_f32 v64, v64, v65\n\t"
        "v_exp_f32 v68, v68\n\t"
        "v_cvt_pk_bf16_f32 v65, v66, v67\n\t"
        "v_mfma_f32_32x32x8_bf16 v[80:95], v[146:147], v[126:127], 0\n\t"
        "v_exp_f32 v69, v69\n\t"
        "v_exp_f32 v70, v70\n\t"
        "v_exp_f32 v71, v71\n\t"
        "v_cvt_pk_bf16_f32 v66, v68, v69\n\t"
        "v_exp_f32 v72, v72\n\t"
        "v_cvt_pk_bf16_f32 v67, v70, v71\n\t"
        "v_mfma_f32_32x32x16_bf16 v[80:95], v[140:143], v[108:111], v[80:95]\n\t"
        "buffer_load_dwordx4 v[136:139], %[ka], %[srd], %[sk] offen offset:3072 sc1\n\t"
        "v_exp_f32 v73, v73\n\t"
        "v_exp_f32 v74, v74\n\t"
        "v_exp_f32 v75, v75\n\t"
        "v_cvt_pk_bf16_f32 v72, v72, v73\n\t"
        "v_exp_f32 v76, v76\n\t"
        "v_cvt_pk_bf16_f32 v73, v74, v75\n\t"
        "v_mfma_f32_32x32x16_bf16 v[32:47], v[172:175], v[64:67], v[32:47]\n\t"
        "v_exp_f32 v77, v77\n\t"
        "v_exp_f32 v78, v78\n\t"
        "v_exp_f32 v79, v79\n\t"
        "v_cvt_pk_bf16_f32 v74, v76, v77\n\t"
        "v_cvt_pk_bf16_f32 v75, v78, v79\n\t"
        "v_exp_f32 v80, v80\n\t"
        "v_exp_f32 v81, v81\n\t"
        "v_mfma_f32_32x32x16_bf16 v[32:47], v[176:179], v[72:75], v[32:47]\n\t"
        "v_exp_f32 v82, v82\n\t"
        "v_exp_f32 v83, v83\n\t"
        "v_cvt_pk_bf16_f32 v80, v80, v81\n\t"
        "v_exp_f32 v84, v84\n\t"
        "s_waitcnt vmcnt(4)\n\t"
        "v_cvt_pk_bf16_f32 v81, v82, v83\n\t"
        "v_mfma_f32_32x32x8_bf16 v[64:79], v[144:145], v[112:113], 0\n\t"
        "v_exp_f32 v85, v85\n\t"
        "v_exp_f32 v86, v86\n\t"
        "v_exp_f32 v87, v87\n\t"
        "v_cvt_pk_bf16_f32 v82, v84, v85\n\t"
        "v_exp_f32 v88, v88\n\t"
        "v_cvt_pk_bf16_f32 v83, v86, v87\n\t"
        "v_mfma_f32_32x32x16_bf16 v[64:79], v[128:131], v[96:99], v[64:79]\n\t"
        "v_exp_f32 v89, v89\n\t"
        "v_exp_f32 v90, v90\n\t"
        "v_exp_f32 v91, v91\n\t"
        "v_cvt_pk_bf16_f32 v88, v88, v89\n\t"
        "v_exp_f32 v92, v92\n\t"
        "v_cvt_pk_bf16_f32 v89, v90, v91\n\t"
        "v_mfma_f32_32x32x16_bf16 v[48:63], v[172:175], v[80:83], v[48:63]\n\t"
        "s_add_u32 %[sk], %[sk], 2048\n\t"
        "v_exp_f32 v93, v93\n\t"
        "v_exp_f32 v94, v94\n\t"
        "s_sub_u32 %[cnt], %[cnt], 1\n\t"
        "v_exp_f32 v95, v95\n\t"
        "v_cvt_pk_bf16_f32 v90, v92, v93\n\t"
        "s_cmp_lg_u32 %[cnt], 0\n\t"
        "v_cvt_pk_bf16_f32 v91, v94, v95\n\t"
        "s_cbranch_scc1 1b\n\t"
        "v_exp_f32 v64, v64\n\t"
        "v_exp_f32 v65, v65\n\t"
        "v_mfma_f32_32x32x16_bf16 v[48:63], v[176:179], v[88:91], v[48:63]\n\t"
        "v_exp_f32 v66, v66\n\t"
        "v_exp_f32 v67, v67\n\t"
        "v_cvt_pk_bf16_f32 v64, v64, v65\n\t"
        "v_exp_f32 v68, v68\n\t"
        "v_cvt_pk_bf16_f32 v65, v66, v67\n\t"
        "v_mfma_f32_32x32x8_bf16 v[80:95], v[144:145], v[114:115], 0\n\t"
        "v_exp_f32 v69, v69\n\t"
        "v_exp_f32 v70, v70\n\t"
        "v_exp_f32 v71, v71\n\t"
        "v_cvt_pk_bf16_f32 v66, v68, v69\n\t"
        "v_exp_f32 v72, v72\n\t"
        "v_cvt_pk_bf16_f32 v67, v70, v71\n\t"
        "v_mfma_f32_32x32x16_bf16 v[80:95], v[128:131], v[100:103], v[80:95]\n\t"
        "buffer_load_dwordx4 v[164:167], %[vo], %[srd], %[sv] offen offset:0 sc1\n\t"
        "v_exp_f32 v73, v73\n\t"
        "v_exp_f32 v74, v74\n\t"
        "buffer_load_dwordx4 v[168:171], %[vo], %[srd], %[sv] offen offset:2176 sc1\n\t"
        "v_exp_f32 v75, v75\n\t"
        "v_cvt_pk_bf16_f32 v72, v72, v73\n\t"
        "v_exp_f32 v76, v76\n\t"
        "s_add_u32 %[sv], %[sv], 8704\n\t"
        "v_cvt_pk_bf16_f32 v73, v74, v75\n\t"
        "v_mfma_f32_32x32x16_bf16 v[0:15], v[148:151], v[64:67], v[0:15]\n\t"
        "v_exp_f32 v77, v77\n\t"
        "v_exp_f32 v78, v78\n\t"
        "v_exp_f32 v79, v79\n\t"
        "v_cvt_pk_bf16_f32 v74, v76, v77\n\t"
        "v_cvt_pk_bf16_f32 v75, v78, v79\n\t"
        "v_exp_f32 v80, v80\n\t"
        "v_exp_f32 v81, v81\n\t"
        "v_mfma_f32_32x32x16_bf16 v[0:15], v[152:155], v[72:75], v[0:15]\n\t"
        "v_exp_f32 v82, v82\n\t"
        "v_exp_f32 v83, v83\n\t"
        "v_cvt_pk_bf16_f32 v80, v80, v81\n\t"
        "v_exp_f32 v84, v84\n\t"
        "v_cvt_pk_bf16_f32 v81, v82, v83\n\t"
        "v_mfma_f32_32x32x8_bf16 v[64:79], v[144:145], v[116:117], 0\n\t"
        "v_exp_f32 v85, v85\n\t"
        "v_exp_f32 v86, v86\n\t"
        "v_exp_f32 v87, v87\n\t"
        "v_cvt_pk_bf16_f32 v82, v84, v85\n\t"
        "v_exp_f32 v88, v88\n\t"
        "v_cvt_pk_bf16_f32 v83, v86, v87\n\t"
        "v_mfma_f32_32x32x16_bf16 v[64:79], v[128:131], v[104:107], v[64:79]\n\t"
        "buffer_load_dwordx2 v[146:147], %[kb], %[srd], %[sk] offen offset:1024 sc1\n\t"
        "v_exp_f32 v89, v89\n\t"
        "v_exp_f32 v90, v90\n\t"
        "v_exp_f32 v91, v91\n\t"
        "v_cvt_pk_bf16_f32 v88, v88, v89\n\t"
        "v_exp_f32 v92, v92\n\t"
        "v_cvt_pk_bf16_f32 v89, v90, v91\n\t"
        "v_mfma_f32_32x32x16_bf16 v[16:31], v[148:151], v[80:83], v[16:31]\n\t"
        "v_exp_f32 v93, v93\n\t"
        "v_exp_f32 v94, v94\n\t"
        "v_exp_f32 v95, v95\n\t"
        "v_cvt_pk_bf16_f32 v90, v92, v93\n\t"
        "v_cvt_pk_bf16_f32 v91, v94, v95\n\t"
        "v_exp_f32 v64, v64\n\t"
        "v_exp_f32 v65, v65\n\t"
        "v_mfma_f32_32x32x16_bf16 v[16:31], v[152:155], v[88:91], v[16:31]\n\t"
        "v_exp_f32 v66, v66\n\t"
        "v_exp_f32 v67, v67\n\t"
        "v_cvt_pk_bf16_f32 v64, v64, v65\n\t"
        "v_exp_f32 v68, v68\n\t"
        "v_cvt_pk_bf16_f32 v65, v66, v67\n\t"
        "v_mfma_f32_32x32x8_bf16 v[80:95], v[144:145], v[118:119], 0\n\t"
        "v_exp_f32 v69, v69\n\t"
        "v_exp_f32 v70, v70\n\t"
        "v_exp_f32 v71, v71\n\t"
        "v_cvt_pk_bf16_f32 v66, v68, v69\n\t"
        "v_exp_f32 v72, v72\n\t"
        "v_cvt_pk_bf16_f32 v67, v70, v71\n\t"
        "v_mfma_f32_32x32x16_bf16 v[80:95], v[128:131], v[108:111], v[80:95]\n\t"
        "buffer_load_dwordx4 v[140:143], %[ka], %[srd], %[sk] offen offset:1536 sc1\n\t"
        "v_exp_f32 v73, v73\n\t"
        "v_exp_f32 v74, v74\n\t"
        "v_exp_f32 v75, v75\n\t"
        "v_cvt_pk_bf16_f32 v72, v72, v73\n\t"
        "v_exp_f32 v76, v76\n\t"
        "v_cvt_pk_bf16_f32 v73, v74, v75\n\t"
        "v_mfma_f32_32x32x16_bf16 v[32:47], v[148:151], v[64:67], v[32:47]\n\t"
        "v_exp_f32 v77, v77\n\t"
        "v_exp_f32 v78, v78\n\t"
        "v_exp_f32 v79, v79\n\t"
        "v_cvt_pk_bf16_f32 v74, v76, v77\n\t"
        "v_cvt_pk_bf16_f32 v75, v78, v79\n\t"
        "v_exp_f32 v80, v80\n\t"
        "v_exp_f32 v81, v81\n\t"
        "v_mfma_f32_32x32x16_bf16 v[32:47], v[152:155], v[72:75], v[32:47]\n\t"
        "v_exp_f32 v82, v82\n\t"
        "v_exp_f32 v83, v83\n\t"
        "v_cvt_pk_bf16_f32 v80, v80, v81\n\t"
        "v_exp_f32 v84, v84\n\t"
        "s_waitcnt vmcnt(5)\n\t"
        "v_cvt_pk_bf16_f32 v81, v82, v83\n\t"
        "v_mfma_f32_32x32x8_bf16 v[64:79], v[144:145], v[120:121], 0\n\t"
        "v_exp_f32 v85, v85\n\t"
        "v_exp_f32 v86, v86\n\t"
        "v_exp_f32 v87, v87\n\t"
        "v_cvt_pk_bf16_f32 v82, v84, v85\n\t"
        "v_exp_f32 v88, v88\n\t"
        "v_cvt_pk_bf16_f32 v83, v86, v87\n\t"
        "v_mfma_f32_32x32x16_bf16 v[64:79], v[132:135], v[96:99], v[64:79]\n\t"
        "v_exp_f32 v89, v89\n\t"
        "v_exp_f32 v90, v90\n\t"
        "v_exp_f32 v91, v91\n\t"
        "v_cvt_pk_bf16_f32 v88, v88, v89\n\t"
        "v_exp_f32 v92, v92\n\t"
        "v_cvt_pk_bf16_f32 v89, v90, v91\n\t"
        "v_mfma_f32_32x32x16_bf16 v[48:63], v[148:151], v[80:83], v[48:63]\n\t"
        "v_exp_f32 v93, v93\n\t"
        "v_exp_f32 v94, v94\n\t"
        "v_exp_f32 v95, v95\n\t"
        "v_cvt_pk_bf16_f32 v90, v92, v93\n\t"
        "v_cvt_pk_bf16_f32 v91, v94, v95\n\t"
        "v_exp_f32 v64, v64\n\t"
        "v_exp_f32 v65, v65\n\t"
        "v_mfma_f32_32x32x16_bf16 v[48:63], v[152:155], v[88:91], v[48:63]\n\t"
        "v_exp_f32 v66, v66\n\t"
        "v_exp_f32 v67, v67\n\t"
        "v_cvt_pk_bf16_f32 v64, v64, v65\n\t"
        "v_exp_f32 v68, v68\n\t"
        "v_cvt_pk_bf16_f32 v65, v66, v67\n\t"
        "v_mfma_f32_32x32x8_bf16 v[80:95], v[144:145], v[122:123], 0\n\t"
        "v_exp_f32 v69, v69\n\t"
        "v_exp_f32 v70, v70\n\t"
        "v_exp_f32 v71, v71\n\t"
        "v_cvt_pk_bf16_f32 v66, v68, v69\n\t"
        "v_exp_f32 v72, v72\n\t"
        "v_cvt_pk_bf16_f32 v67, v70, v71\n\t"
        "v_mfma_f32_32x32x16_bf16 v[80:95], v[132:135], v[100:103], v[80:95]\n\t"
        "buffer_load_dwordx4 v[172:175], %[vo], %[srd], %[sv] offen offset:0 sc1\n\t"
        "v_exp_f32 v73, v73\n\t"
        "v_exp_f32 v74, v74\n\t"
        "buffer_load_dwordx4 v[176:179], %[vo], %[srd], %[sv] offen offset:2176 sc1\n\t"
        "v_exp_f32 v75, v75\n\t"
        "v_cvt_pk_bf16_f32 v72, v72, v73\n\t"
        "v_exp_f32 v76, v76\n\t"
        "s_add_u32 %[sv], %[sv], 8704\n\t"
        "v_cvt_pk_bf16_f32 v73, v74, v75\n\t"
        "v_mfma_f32_32x32x16_bf16 v[0:15], v[156:159], v[64:67], v[0:15]\n\t"
        "v_exp_f32 v77, v77\n\t"
        "v_exp_f32 v78, v78\n\t"
        "v_exp_f32 v79, v79\n\t"
        "v_cvt_pk_bf16_f32 v74, v76, v77\n\t"
        "v_cvt_pk_bf16_f32 v75, v78, v79\n\t"
        "v_exp_f32 v80, v80\n\t"
        "v_exp_f32 v81, v81\n\t"
        "v_mfma_f32_32x32x16_bf16 v[0:15], v[160:163], v[72:75], v[0:15]\n\t"
        "v_exp_f32 v82, v82\n\t"
        "v_exp_f32 v83, v83\n\t"
        "v_cvt_pk_bf16_f32 v80, v80, v81\n\t"
        "v_exp_f32 v84, v84\n\t"
        "v_cvt_pk_bf16_f32 v81, v82, v83\n\t"
        "v_mfma_f32_32x32x8_bf16 v[64:79], v[144:145], v[124:125], 0\n\t"
        "v_exp_f32 v85, v85\n\t"
        "v_exp_f32 v86, v86\n\t"
        "v_exp_f32 v87, v87\n\t"
        "v_cvt_pk_bf16_f32 v82, v84, v85\n\t"
        "v_exp_f32 v88, v88\n\t"
        "v_cvt_pk_bf16_f32 v83, v86, v87\n\t"
        "v_mfma_f32_32x32x16_bf16 v[64:79], v[132:135], v[104:107], v[64:79]\n\t"
        "v_exp_f32 v89, v89\n\t"
        "v_exp_f32 v90, v90\n\t"
        "v_exp_f32 v91, v91\n\t"
        "v_cvt_pk_bf16_f32 v88, v88, v89\n\t"
        "v_exp_f32 v92, v92\n\t"
        "v_cvt_pk_bf16_f32 v89, v90, v91\n\t"
        "v_mfma_f32_32x32x16_bf16 v[16:31], v[156:159], v[80:83], v[16:31]\n\t"
        "v_exp_f32 v93, v93\n\t"
        "v_exp_f32 v94, v94\n\t"
        "v_exp_f32 v95, v95\n\t"
        "v_cvt_pk_bf16_f32 v90, v92, v93\n\t"
        "v_cvt_pk_bf16_f32 v91, v94, v95\n\t"
        "v_exp_f32 v64, v64\n\t"
        "v_exp_f32 v65, v65\n\t"
        "v_mfma_f32_32x32x16_bf16 v[16:31], v[160:163], v[88:91], v[16:31]\n\t"
        "v_exp_f32 v66, v66\n\t"
        "v_exp_f32 v67, v67\n\t"
        "v_cvt_pk_bf16_f32 v64, v64, v65\n\t"
        "v_exp_f32 v68, v68\n\t"
        "v_cvt_pk_bf16_f32 v65, v66, v67\n\t"
        "v_mfma_f32_32x32x8_bf16 v[80:95], v[144:145], v[126:127], 0\n\t"
        "v_exp_f32 v69, v69\n\t"
        "v_exp_f32 v70, v70\n\t"
        "v_exp_f32 v71, v71\n\t"
        "v_cvt_pk_bf16_f32 v66, v68, v69\n\t"
        "v_exp_f32 v72, v72\n\t"
        "v_cvt_pk_bf16_f32 v67, v70, v71\n\t"
        "v_mfma_f32_32x32x16_bf16 v[80:95], v[132:135], v[108:111], v[80:95]\n\t"
        "v_exp_f32 v73, v73\n\t"
        "v_exp_f32 v74, v74\n\t"
        "v_exp_f32 v75, v75\n\t"
        "v_cvt_pk_bf16_f32 v72, v72, v73\n\t"
        "v_exp_f32 v76, v76\n\t"
        "v_cvt_pk_bf16_f32 v73, v74, v75\n\t"
        "v_mfma_f32_32x32x16_bf16 v[32:47], v[156:159], v[64:67], v[32:47]\n\t"
        "v_exp_f32 v77, v77\n\t"
        "v_exp_f32 v78, v78\n\t"
        "v_exp_f32 v79, v79\n\t"
        "v_cvt_pk_bf16_f32 v74, v76, v77\n\t"
        "v_cvt_pk_bf16_f32 v75, v78, v79\n\t"
        "v_exp_f32 v80, v80\n\t"
        "v_exp_f32 v81, v81\n\t"
        "v_mfma_f32_32x32x16_bf16 v[32:47], v[160:163], v[72:75], v[32:47]\n\t"
        "v_exp_f32 v82, v82\n\t"
        "v_exp_f32 v83, v83\n\t"
        "v_cvt_pk_bf16_f32 v80, v80, v81\n\t"
        "v_exp_f32 v84, v84\n\t"
        "s_waitcnt vmcnt(3)\n\t"
        "v_cvt_pk_bf16_f32 v81, v82, v83\n\t"
        "v_mfma_f32_32x32x8_bf16 v[64:79], v[146:147], v[112:113], 0\n\t"
        "v_exp_f32 v85, v85\n\t"
        "v_exp_f32 v86, v86\n\t"
        "v_exp_f32 v87, v87\n\t"
        "v_cvt_pk_bf16_f32 v82, v84, v85\n\t"
        "v_exp_f32 v88, v88\n\t"
        "v_cvt_pk_bf16_f32 v83, v86, v87\n\t"
        "v_mfma_f32_32x32x16_bf16 v[64:79], v[136:139], v[96:99], v[64:79]\n\t"
        "v_exp_f32 v89, v89\n\t"
        "v_exp_f32 v90, v90\n\t"
        "v_exp_f32 v91, v91\n\t"
        "v_cvt_pk_bf16_f32 v88, v88, v89\n\t"
        "v_exp_f32 v92, v92\n\t"
        "v_cvt_pk_bf16_f32 v89, v90, v91\n\t"
        "v_mfma_f32_32x32x16_bf16 v[48:63], v[156:159], v[80:83], v[48:63]\n\t"
        "v_exp_f32 v93, v93\n\t"
        "v_exp_f32 v94, v94\n\t"
        "v_exp_f32 v95, v95\n\t"
        "v_cvt_pk_bf16_f32 v90, v92, v93\n\t"
        "v_cvt_pk_bf16_f32 v91, v94, v95\n\t"
        "v_exp_f32 v64, v64\n\t"
        "v_exp_f32 v65, v65\n\t"
        "v_mfma_f32_32x32x16_bf16 v[48:63], v[160:163], v[88:91], v[48:63]\n\t"
        "v_exp_f32 v66, v66\n\t"
        "v_exp_f32 v67, v67\n\t"
        "v_cvt_pk_bf16_f32 v64, v64, v65\n\t"
        "v_exp_f32 v68, v68\n\t"
        "v_cvt_pk_bf16_f32 v65, v66, v67\n\t"
        "v_mfma_f32_32x32x8_bf16 v[80:95], v[146:147], v[114:115], 0\n\t"
        "v_exp_f32 v69, v69\n\t"
        "v_exp_f32 v70, v70\n\t"
        "v_exp_f32 v71, v71\n\t"
        "v_cvt_pk_bf16_f32 v66, v68, v69\n\t"
        "v_exp_f32 v72, v72\n\t"
        "v_cvt_pk_bf16_f32 v67, v70, v71\n\t"
        "v_mfma_f32_32x32x16_bf16 v[80:95], v[136:139], v[100:103], v[80:95]\n\t"
        "v_exp_f32 v73, v73\n\t"
        "v_exp_f32 v74, v74\n\t"
        "v_exp_f32 v75, v75\n\t"
        "v_cvt_pk_bf16_f32 v72, v72, v73\n\t"
        "v_exp_f32 v76, v76\n\t"
        "v_cvt_pk_bf16_f32 v73, v74, v75\n\t"
        "v_mfma_f32_32x32x16_bf16 v[0:15], v[164:167], v[64:67], v[0:15]\n\t"
        "v_exp_f32 v77, v77\n\t"
        "v_exp_f32 v78, v78\n\t"
        "v_exp_f32 v79, v79\n\t"
        "v_cvt_pk_bf16_f32 v74, v76, v77\n\t"
        "v_cvt_pk_bf16_f32 v75, v78, v79\n\t"
        "v_exp_f32 v80, v80\n\t"
        "v_exp_f32 v81, v81\n\t"
        "v_mfma_f32_32x32x16_bf16 v[0:15], v[168:171], v[72:75], v[0:15]\n\t"
        "v_exp_f32 v82, v82\n\t"
        "v_exp_f32 v83, v83\n\t"
        "v_cvt_pk_bf16_f32 v80, v80, v81\n\t"
        "v_exp_f32 v84, v84\n\t"
        "v_cvt_pk_bf16_f32 v81, v82, v83\n\t"
        "v_mfma_f32_32x32x8_bf16 v[64:79], v[146:147], v[116:117], 0\n\t"
        "v_exp_f32 v85, v85\n\t"
        "v_exp_f32 v86, v86\n\t"
        "v_exp_f32 v87, v87\n\t"
        "v_cvt_pk_bf16_f32 v82, v84, v85\n\t"
        "v_exp_f32 v88, v88\n\t"
        "v_cvt_pk_bf16_f32 v83, v86, v87\n\t"
        "v_mfma_f32_32x32x16_bf16 v[64:79], v[136:139], v[104:107], v[64:79]\n\t"
        "v_exp_f32 v89, v89\n\t"
        "v_exp_f32 v90, v90\n\t"
        "v_exp_f32 v91, v91\n\t"
        "v_cvt_pk_bf16_f32 v88, v88, v89\n\t"
        "v_exp_f32 v92, v92\n\t"
        "v_cvt_pk_bf16_f32 v89, v90, v91\n\t"
        "v_mfma_f32_32x32x16_bf16 v[16:31], v[164:167], v[80:83], v[16:31]\n\t"
        "v_exp_f32 v93, v93\n\t"
        "v_exp_f32 v94, v94\n\t"
        "v_exp_f32 v95, v95\n\t"
        "v_cvt_pk_bf16_f32 v90, v92, v93\n\t"
        "v_cvt_pk_bf16_f32 v91, v94, v95\n\t"
        "v_exp_f32 v64, v64\n\t"
        "v_exp_f32 v65, v65\n\t"
        "v_mfma_f32_32x32x16_bf16 v[16:31], v[168:171], v[88:91], v[16:31]\n\t"
        "v_exp_f32 v66, v66\n\t"
        "v_exp_f32 v67, v67\n\t"
        "v_cvt_pk_bf16_f32 v64, v64, v65\n\t"
        "v_exp_f32 v68, v68\n\t"
        "v_cvt_pk_bf16_f32 v65, v66, v67\n\t"
        "v_mfma_f32_32x32x8_bf16 v[80:95], v[146:147], v[118:119], 0\n\t"
        "v_exp_f32 v69, v69\n\t"
        "v_exp_f32 v70, v70\n\t"
        "v_exp_f32 v71, v71\n\t"
        "v_cvt_pk_bf16_f32 v66, v68, v69\n\t"
        "v_exp_f32 v72, v72\n\t"
        "v_cvt_pk_bf16_f32 v67, v70, v71\n\t"
        "v_mfma_f32_32x32x16_bf16 v[80:95], v[136:139], v[108:111], v[80:95]\n\t"
        "v_exp_f32 v73, v73\n\t"
        "v_exp_f32 v74, v74\n\t"
        "v_exp_f32 v75, v75\n\t"
        "v_cvt_pk_bf16_f32 v72, v72, v73\n\t"
        "v_exp_f32 v76, v76\n\t"
        "v_cvt_pk_bf16_f32 v73, v74, v75\n\t"
        "v_mfma_f32_32x32x16_bf16 v[32:47], v[164:167], v[64:67], v[32:47]\n\t"
        "v_exp_f32 v77, v77\n\t"
        "v_exp_f32 v78, v78\n\t"
        "v_exp_f32 v79, v79\n\t"
        "v_cvt_pk_bf16_f32 v74, v76, v77\n\t"
        "v_cvt_pk_bf16_f32 v75, v78, v79\n\t"
        "v_exp_f32 v80, v80\n\t"
        "v_exp_f32 v81, v81\n\t"
        "v_mfma_f32_32x32x16_bf16 v[32:47], v[168:171], v[72:75], v[32:47]\n\t"
        "v_exp_f32 v82, v82\n\t"
        "v_exp_f32 v83, v83\n\t"
        "v_cvt_pk_bf16_f32 v80, v80, v81\n\t"
        "v_exp_f32 v84, v84\n\t"
        "s_waitcnt vmcnt(0)\n\t"
        "v_cvt_pk_bf16_f32 v81, v82, v83\n\t"
        "v_mfma_f32_32x32x8_bf16 v[64:79], v[146:147], v[120:121], 0\n\t"
        "v_exp_f32 v85, v85\n\t"
        "v_exp_f32 v86, v86\n\t"
        "v_exp_f32 v87, v87\n\t"
        "v_cvt_pk_bf16_f32 v82, v84, v85\n\t"
        "v_exp_f32 v88, v88\n\t"
        "v_cvt_pk_bf16_f32 v83, v86, v87\n\t"
        "v_mfma_f32_32x32x16_bf16 v[64:79], v[140:143], v[96:99], v[64:79]\n\t"
        "v_exp_f32 v89, v89\n\t"
        "v_exp_f32 v90, v90\n\t"
        "v_exp_f32 v91, v91\n\t"
        "v_cvt_pk_bf16_f32 v88, v88, v89\n\t"
        "v_exp_f32 v92, v92\n\t"
        "v_cvt_pk_bf16_f32 v89, v90, v91\n\t"
        "v_mfma_f32_32x32x16_bf16 v[48:63], v[164:167], v[80:83], v[48:63]\n\t"
        "v_exp_f32 v93, v93\n\t"
        "v_exp_f32 v94, v94\n\t"
        "v_exp_f32 v95, v95\n\t"
        "v_cvt_pk_bf16_f32 v90, v92, v93\n\t"
        "v_cvt_pk_bf16_f32 v91, v94, v95\n\t"
        "v_exp_f32 v64, v64\n\t"
        "v_exp_f32 v65, v65\n\t"
        "v_mfma_f32_32x32x16_bf16 v[48:63], v[168:171], v[88:91], v[48:63]\n\t"
        "v_exp_f32 v66, v66\n\t"
        "v_exp_f32 v67, v67\n\t"
        "v_cvt_pk_bf16_f32 v64, v64, v65\n\t"
        "v_exp_f32 v68, v68\n\t"
        "v_cvt_pk_bf16_f32 v65, v66, v67\n\t"
        "v_mfma_f32_32x32x8_bf16 v[80:95], v[146:147], v[122:123], 0\n\t"
        "v_exp_f32 v69, v69\n\t"
        "v_exp_f32 v70, v70\n\t"
        "v_exp_f32 v71, v71\n\t"
        "v_cvt_pk_bf16_f32 v66, v68, v69\n\t"
        "v_exp_f32 v72, v72\n\t"
        "v_cvt_pk_bf16_f32 v67, v70, v71\n\t"
        "v_mfma_f32_32x32x16_bf16 v[80:95], v[140:143], v[100:103], v[80:95]\n\t"
        "v_exp_f32 v73, v73\n\t"
        "v_exp_f32 v74, v74\n\t"
        "v_exp_f32 v75, v75\n\t"
        "v_cvt_pk_bf16_f32 v72, v72, v73\n\t"
        "v_exp_f32 v76, v76\n\t"
        "v_cvt_pk_bf16_f32 v73, v74, v75\n\t"
        "v_mfma_f32_32x32x16_bf16 v[0:15], v[172:175], v[64:67], v[0:15]\n\t"
        "v_exp_f32 v77, v77\n\t"
        "v_exp_f32 v78, v78\n\t"
        "v_exp_f32 v79, v79\n\t"
        "v_cvt_pk_bf16_f32 v74, v76, v77\n\t"
        "v_cvt_pk_bf16_f32 v75, v78, v79\n\t"
        "v_exp_f32 v80, v80\n\t"
        "v_exp_f32 v81, v81\n\t"
        "v_mfma_f32_32x32x16_bf16 v[0:15], v[176:179], v[72:75], v[0:15]\n\t"
        "v_exp_f32 v82, v82\n\t"
        "v_exp_f32 v83, v83\n\t"
        "v_cvt_pk_bf16_f32 v80, v80, v81\n\t"
        "v_exp_f32 v84, v84\n\t"
        "v_cvt_pk_bf16_f32 v81, v82, v83\n\t"
        "v_mfma_f32_32x32x8_bf16 v[64:79], v[146:147], v[124:125], 0\n\t"
        "v_exp_f32 v85, v85\n\t"
        "v_exp_f32 v86, v86\n\t"
        "v_exp_f32 v87, v87\n\t"
        "v_cvt_pk_bf16_f32 v82, v84, v85\n\t"
        "v_exp_f32 v88, v88\n\t"
        "v_cvt_pk_bf16_f32 v83, v86, v87\n\t"
        "v_mfma_f32_32x32x16_bf16 v[64:79], v[140:143], v[104:107], v[64:79]\n\t"
        "v_exp_f32 v89, v89\n\t"
        "v_exp_f32 v90, v90\n\t"
        "v_exp_f32 v91, v91\n\t"
        "v_cvt_pk_bf16_f32 v88, v88, v89\n\t"
        "v_exp_f32 v92, v92\n\t"
        "v_cvt_pk_bf16_f32 v89, v90, v91\n\t"
        "v_mfma_f32_32x32x16_bf16 v[16:31], v[172:175], v[80:83], v[16:31]\n\t"
        "v_exp_f32 v93, v93\n\t"
        "v_exp_f32 v94, v94\n\t"
        "v_exp_f32 v95, v95\n\t"
        "v_cvt_pk_bf16_f32 v90, v92, v93\n\t"
        "v_cvt_pk_bf16_f32 v91, v94, v95\n\t"
        "v_exp_f32 v64, v64\n\t"
        "v_exp_f32 v65, v65\n\t"
        "v_mfma_f32_32x32x16_bf16 v[16:31], v[176:179], v[88:91], v[16:31]\n\t"
        "v_exp_f32 v66, v66\n\t"
        "v_exp_f32 v67, v67\n\t"
        "v_cvt_pk_bf16_f32 v64, v64, v65\n\t"
        "v_exp_f32 v68, v68\n\t"
        "v_cvt_pk_bf16_f32 v65, v66, v67\n\t"
        "v_mfma_f32_32x32x8_bf16 v[80:95], v[146:147], v[126:127], 0\n\t"
        "v_exp_f32 v69, v69\n\t"
        "v_exp_f32 v70, v70\n\t"
        "v_exp_f32 v71, v71\n\t"
        "v_cvt_pk_bf16_f32 v66, v68, v69\n\t"
        "v_exp_f32 v72, v72\n\t"
        "v_cvt_pk_bf16_f32 v67, v70, v71\n\t"
        "v_mfma_f32_32x32x16_bf16 v[80:95], v[140:143], v[108:111], v[80:95]\n\t"
        "v_exp_f32 v73, v73\n\t"
        "v_exp_f32 v74, v74\n\t"
        "v_exp_f32 v75, v75\n\t"
        "v_cvt_pk_bf16_f32 v72, v72, v73\n\t"
        "v_exp_f32 v76, v76\n\t"
        "v_cvt_pk_bf16_f32 v73, v74, v75\n\t"
        "v_mfma_f32_32x32x16_bf16 v[32:47], v[172:175], v[64:67], v[32:47]\n\t"
        "v_exp_f32 v77, v77\n\t"
        "v_exp_f32 v78, v78\n\t"
        "v_exp_f32 v79, v79\n\t"
        "v_cvt_pk_bf16_f32 v74, v76, v77\n\t"
        "v_cvt_pk_bf16_f32 v75, v78, v79\n\t"
        "v_exp_f32 v80, v80\n\t"
        "v_exp_f32 v81, v81\n\t"
        "v_mfma_f32_32x32x16_bf16 v[32:47], v[176:179], v[72:75], v[32:47]\n\t"
        "v_exp_f32 v82, v82\n\t"
        "v_exp_f32 v83, v83\n\t"
        "v_cvt_pk_bf16_f32 v80, v80, v81\n\t"
        "v_exp_f32 v84, v84\n\t"
        "v_cvt_pk_bf16_f32 v81, v82, v83\n\t"
        "v_exp_f32 v85, v85\n\t"
        "v_exp_f32 v86, v86\n\t"
        "v_exp_f32 v87, v87\n\t"
        "v_cvt_pk_bf16_f32 v82, v84, v85\n\t"
        "v_exp_f32 v88, v88\n\t"
        "v_cvt_pk_bf16_f32 v83, v86, v87\n\t"
        "v_exp_f32 v89, v89\n\t"
        "v_exp_f32 v90, v90\n\t"
        "v_exp_f32 v91, v91\n\t"
        "v_cvt_pk_bf16_f32 v88, v88, v89\n\t"
        "v_exp_f32 v92, v92\n\t"
        "v_cvt_pk_bf16_f32 v89, v90, v91\n\t"
        "v_mfma_f32_32x32x16_bf16 v[48:63], v[172:175], v[80:83], v[48:63]\n\t"
        "v_exp_f32 v93, v93\n\t"
        "v_exp_f32 v94, v94\n\t"
        "v_exp_f32 v95, v95\n\t"
        "v_cvt_pk_bf16_f32 v90, v92, v93\n\t"
        "v_cvt_pk_bf16_f32 v91, v94, v95\n\t"
        "s_cmp_lg_u32 0, 0\n\t"
        "s_nop 0\n\t"
        "v_mfma_f32_32x32x16_bf16 v[48:63], v[176:179], v[88:91], v[48:63]\n\t"
        "s_nop 11\n\t"
        : "=&{v[0:15]}"(o[0]), "=&{v[16:31]}"(o[1]), "=&{v[32:47]}"(o[2]), "=&{v[48:63]}"(o[3]), [sk] "+s"(sk), [sv] "+s"(sv), [cnt] "+s"(cnt)
        : ATT_IN
        : "memory", "scc", "v64", "v65", "v66", "v67", "v68", "v69", "v70", "v71", "v72", "v73", "v74", "v75", "v76", "v77", "v78", "v79", "v80", "v81", "v82", "v83", "v84", "v85", "v86", "v87", "v88", "v89", "v90", "v91", "v92", "v93", "v94", "v95", "v96", "v97", "v98", "v99", "v100", "v101", "v102", "v103", "v104", "v105", "v106", "v107", "v108", "v109", "v110", "v111", "v112", "v113", "v114", "v115", "v116", "v117", "v118", "v119", "v120", "v121", "v122", "v123", "v124", "v125", "v126", "v127", "v128", "v129", "v130", "v131", "v132", "v133", "v134", "v135", "v136", "v137", "v138", "v139", "v140", "v141", "v142", "v143", "v144", "v145", "v146", "v147", "v148", "v149", "v150", "v151", "v152", "v153", "v154", "v155", "v156", "v157", "v158", "v159", "v160", "v161", "v162", "v163", "v164", "v165", "v166", "v167", "v168", "v169", "v170", "v171", "v172", "v173", "v174", "v175", "v176", "v177", "v178", "v179", "v180");
}
DI void attend_finish(char* ws, unsigned sk, unsigned sv, unsigned sq0, unsigned sq1, const AttLane& A, unsigned pw, unsigned pr, unsigned ca, f32x16& o2, f32x16& o3) {
    const u32x4 srd = mksrd(ws + WS_BR, WS_BR);
    unsigned cnt = 3;
    asm volatile(
        "buffer_load_dwordx4 v[128:131], %[ka], %[srd], %[sk] offen offset:0 sc1\n\t"
        "buffer_load_dwordx2 v[144:145], %[kb], %[srd], %[sk] offen offset:0 sc1\n\t"
        "buffer_load_dwordx4 v[96:99], %[qa], %[srd], %[sq0] offen offset:0 sc1\n\t"
        "buffer_load_dwordx2 v[112:113], %[qb0], %[srd], %[sq0] offen offset:0 sc1\n\t"
        "buffer_load_dwordx2 v[120:121], %[qb1], %[srd], %[sq0] offen offset:0 sc1\n\t"
        "buffer_load_dwordx4 v[100:103], %[qa], %[srd], %[sq0] offen offset:512 sc1\n\t"
        "buffer_load_dwordx2 v[114:115], %[qb0], %[srd], %[sq0] offen offset:512 sc1\n\t"
        "buffer_load_dwordx2 v[122:123], %[qb1], %[srd], %[sq0] offen offset:512 sc1\n\t"
        "buffer_load_dwordx4 v[148:151], %[vo], %[srd], %[sv] offen offset:0 sc1\n\t"
        "buffer_load_dwordx4 v[152:155], %[vo], %[srd], %[sv] offen offset:2176 sc1\n\t"
        "s_add_u32 %[sv], %[sv], 8704\n\t"
        "buffer_load_dwordx4 v[104:107], %[qa], %[srd], %[sq1] offen offset:0 sc1\n\t"
        "buffer_load_dwordx2 v[116:117], %[qb0], %[srd], %[sq1] offen offset:0 sc1\n\t"
        "buffer_load_dwordx2 v[124:125], %[qb1], %[srd], %[sq1] offen offset:0 sc1\n\t"
        "buffer_load_dwordx4 v[108:111], %[qa], %[srd], %[sq1] offen offset:512 sc1\n\t"
        "buffer_load_dwordx2 v[118:119], %[qb0], %[srd], %[sq1] offen offset:512 sc1\n\t"
        "buffer_load_dwordx2 v[126:127], %[qb1], %[srd], %[sq1] offen offset:512 sc1\n\t"
        "buffer_load_dwordx4 v[132:135], %[ka], %[srd], %[sk] offen offset:512 sc1\n\t"
        "buffer_load_dwordx4 v[156:159], %[vo], %[srd], %[sv] offen offset:0 sc1\n\t"
        "buffer_load_dwordx4 v[160:163], %[vo], %[srd], %[sv] offen offset:2176 sc1\n\t"
        "s_add_u32 %[sv], %[sv], 8704\n\t"
        "buffer_load_dwordx4 v[136:139], %[ka], %[srd], %[sk] offen offset:1024 sc1\n\t"
        "s_waitcnt vmcnt(15)\n\t"
        "v_mfma_f32_32x32x8_bf16 v[64:79], v[144:145], v[112:113], 0\n\t"
        "v_mfma_f32_32x32x16_bf16 v[64:79], v[128:131], v[96:99], v[64:79]\n\t"
        "s_nop 11\n\t"
        "v_exp_f32 v64, v64\n\t"
        "v_exp_f32 v65, v65\n\t"
        "v_exp_f32 v66, v66\n\t"
        "v_exp_f32 v67, v67\n\t"
        "v_cvt_pk_bf16_f32 v64, v64, v65\n\t"
        "v_exp_f32 v68, v68\n\t"
        "s_waitcnt vmcnt(12)\n\t"
        "v_cvt_pk_bf16_f32 v65, v66, v67\n\t"
        "v_mfma_f32_32x32x8_bf16 v[80:95], v[144:145], v[114:115], 0\n\t"
        "v_exp_f32 v69, v69\n\t"
        "v_exp_f32 v70, v70\n\t"
        "v_exp_f32 v71, v71\n\t"
        "v_cvt_pk_bf16_f32 v66, v68, v69\n\t"
        "v_exp_f32 v72, v72\n\t"
        "v_cvt_pk_bf16_f32 v67, v70, v71\n\t"
        "v_mfma_f32_32x32x16_bf16 v[80:95], v[128:131], v[100:103], v[80:95]\n\t"
        "buffer_load_dwordx4 v[164:167], %[vo], %[srd], %[sv] offen offset:0 sc1\n\t"
        "v_exp_f32 v73, v73\n\t"
        "v_exp_f32 v74, v74\n\t"
        "buffer_load_dwordx4 v[168:171], %[vo], %[srd], %[sv] offen offset:2176 sc1\n\t"
        "v_exp_f32 v75, v75\n\t"
        "v_cvt_pk_bf16_f32 v72, v72, v73\n\t"
        "v_exp_f32 v76, v76\n\t"
        "s_add_u32 %[sv], %[sv], 8704\n\t"
        "v_cvt_pk_bf16_f32 v73, v74, v75\n\t"
        "s_waitcnt vmcnt(12)\n\t"
        "v_mfma_f32_32x32x16_bf16 v[0:15], v[148:151], v[64:67], 0\n\t"
        "v_exp_f32 v77, v77\n\t"
        "v_exp_f32 v78, v78\n\t"
        "v_exp_f32 v79, v79\n\t"
        "v_cvt_pk_bf16_f32 v74, v76, v77\n\t"
        "v_cvt_pk_bf16_f32 v75, v78, v79\n\t"
        "v_exp_f32 v80, v80\n\t"
        "v_exp_f32 v81, v81\n\t"
        "v_mfma_f32_32x32x16_bf16 v[0:15], v[152:155], v[72:75], v[0:15]\n\t"
        "v_exp_f32 v82, v82\n\t"
        "v_exp_f32 v83, v83\n\t"
        "v_cvt_pk_bf16_f32 v80, v80, v81\n\t"
        "v_exp_f32 v84, v84\n\t"
        "s_waitcnt vmcnt(9)\n\t"
        "v_cvt_pk_bf16_f32 v81, v82, v83\n\t"
        "v_mfma_f32_32x32x8_bf16 v[64:79], v[144:145], v[116:117], 0\n\t"
        "v_exp_f32 v85, v85\n\t"
        "v_exp_f32 v86, v86\n\t"
        "v_exp_f32 v87, v87\n\t"
        "v_cvt_pk_bf16_f32 v82, v84, v85\n\t"
        "v_exp_f32 v88, v88\n\t"
        "v_cvt_pk_bf16_f32 v83, v86, v87\n\t"
        "v_mfma_f32_32x32x16_bf16 v[64:79], v[128:131], v[104:107], v[64:79]\n\t"
        "buffer_load_dwordx2 v[146:147], %[kb], %[srd], %[sk] offen offset:1024 sc1\n\t"
        "v_exp_f32 v89, v89\n\t"
        "v_exp_f32 v90, v90\n\t"
        "v_exp_f32 v91, v91\n\t"
        "v_cvt_pk_bf16_f32 v88, v88, v89\n\t"
        "v_exp_f32 v92, v92\n\t"
        "v_cvt_pk_bf16_f32 v89, v90, v91\n\t"
        "v_mfma_f32_32x32x16_bf16 v[16:31], v[148:151], v[80:83], 0\n\t"
        "v_exp_f32 v93, v93\n\t"
        "v_exp_f32 v94, v94\n\t"
        "v_exp_f32 v95, v95\n\t"
        "v_cvt_pk_bf16_f32 v90, v92, v93\n\t"
        "v_cvt_pk_bf16_f32 v91, v94, v95\n\t"
        "v_exp_f32 v64, v64\n\t"
        "v_exp_f32 v65, v65\n\t"
        "v_mfma_f32_32x32x16_bf16 v[16:31], v[152:155], v[88:91], v[16:31]\n\t"
        "v_exp_f32 v66, v66\n\t"
        "v_exp_f32 v67, v67\n\t"
        "v_cvt_pk_bf16_f32 v64, v64, v65\n\t"
        "v_exp_f32 v68, v68\n\t"
        "s_waitcnt vmcnt(7)\n\t"
        "v_cvt_pk_bf16_f32 v65, v66, v67\n\t"
        "v_mfma_f32_32x32x8_bf16 v[80:95], v[144:145], v[118:119], 0\n\t"
        "v_exp_f32 v69, v69\n\t"
        "v_exp_f32 v70, v70\n\t"
        "v_exp_f32 v71, v71\n\t"
        "v_cvt_pk_bf16_f32 v66, v68, v69\n\t"
        "v_exp_f32 v72, v72\n\t"
        "v_cvt_pk_bf16_f32 v67, v70, v71\n\t"
        "v_mfma_f32_32x32x16_bf16 v[80:95], v[128:131], v[108:111], v[80:95]\n\t"
        "buffer_load_dwordx4 v[140:143], %[ka], %[srd], %[sk] offen offset:1536 sc1\n\t"
        "v_exp_f32 v73, v73\n\t"
        "v_exp_f32 v74, v74\n\t"
        "v_exp_f32 v75, v75\n\t"
        "v_cvt_pk_bf16_f32 v72, v72, v73\n\t"
        "v_exp_f32 v76, v76\n\t"
        "v_cvt_pk_bf16_f32 v73, v74, v75\n\t"
        "v_mfma_f32_32x32x16_bf16 v[32:47], v[148:151], v[64:67], 0\n\t"
        "v_exp_f32 v77, v77\n\t"
        "v_exp_f32 v78, v78\n\t"
        "v_exp_f32 v79, v79\n\t"
        "v_cvt_pk_bf16_f32 v74, v76, v77\n\t"
        "v_cvt_pk_bf16_f32 v75, v78, v79\n\t"
        "v_exp_f32 v80, v80\n\t"
        "v_exp_f32 v81, v81\n\t"
        "v_mfma_f32_32x32x16_bf16 v[32:47], v[152:155], v[72:75], v[32:47]\n\t"
        "v_exp_f32 v82, v82\n\t"
        "v_exp_f32 v83, v83\n\t"
        "v_cvt_pk_bf16_f32 v80, v80, v81\n\t"
        "v_exp_f32 v84, v84\n\t"
        "s_waitcnt vmcnt(5)\n\t"
        "v_cvt_pk_bf16_f32 v81, v82, v83\n\t"
        "v_mfma_f32_32x32x8_bf16 v[64:79], v[144:145], v[120:121], 0\n\t"
        "v_exp_f32 v85, v85\n\t"
        "v_exp_f32 v86, v86\n\t"
        "v_exp_f32 v87, v87\n\t"
        "v_cvt_pk_bf16_f32 v82, v84, v85\n\t"
        "v_exp_f32 v88, v88\n\t"
        "v_cvt_pk_bf16_f32 v83, v86, v87\n\t"
        "v_mfma_f32_32x32x16_bf16 v[64:79], v[132:135], v[96:99], v[64:79]\n\t"
        "v_exp_f32 v89, v89\n\t"
        "v_exp_f32 v90, v90\n\t"
        "v_exp_f32 v91, v91\n\t"
        "v_cvt_pk_bf16_f32 v88, v88, v89\n\t"
        "v_exp_f32 v92, v92\n\t"
        "v_cvt_pk_bf16_f32 v89, v90, v91\n\t"
        "v_mfma_f32_32x32x16_bf16 v[48:63], v[148:151], v[80:83], 0\n\t"
        "v_exp_f32 v93, v93\n\t"
        "v_exp_f32 v94, v94\n\t"
        "v_exp_f32 v95, v95\n\t"
        "v_cvt_pk_bf16_f32 v90, v92, v93\n\t"
        "v_cvt_pk_bf16_f32 v91, v94, v95\n\t"
        "s_branch 5f\n\t"
        "1:\n\t"
        "v_exp_f32 v64, v64\n\t"
        "v_exp_f32 v65, v65\n\t"
        "v_mfma_f32_32x32x16_bf16 v[48:63], v[176:179], v[88:91], v[48:63]\n\t"
        "v_exp_f32 v66, v66\n\t"
        "v_exp_f32 v67, v67\n\t"
        "v_cvt_pk_bf16_f32 v64, v64, v65\n\t"
        "v_exp_f32 v68, v68\n\t"
        "v_cvt_pk_bf16_f32 v65, v66, v67\n\t"
        "v_mfma_f32_32x32x8_bf16 v[80:95], v[144:145], v[114:115], 0\n\t"
        "v_exp_f32 v69, v69\n\t"
        "v_exp_f32 v70, v70\n\t"
        "v_exp_f32 v71, v71\n\t"
        "v_cvt_pk_bf16_f32 v66, v68, v69\n\t"
        "v_exp_f32 v72, v72\n\t"
        "v_cvt_pk_bf16_f32 v67, v70, v71\n\t"
        "v_mfma_f32_32x32x16_bf16 v[80:95], v[128:131], v[100:103], v[80:95]\n\t"
        "buffer_load_dwordx4 v[164:167], %[vo], %[srd], %[sv] offen offset:0 sc1\n\t"
        "v_exp_f32 v73, v73\n\t"
        "v_exp_f32 v74, v74\n\t"
        "buffer_load_dwordx4 v[168:171], %[vo], %[srd], %[sv] offen offset:2176 sc1\n\t"
        "v_exp_f32 v75, v75\n\t"
        "v_cvt_pk_bf16_f32 v72, v72, v73\n\t"
        "v_exp_f32 v76, v76\n\t"
        "s_add_u32 %[sv], %[sv], 8704\n\t"
        "v_cvt_pk_bf16_f32 v73, v74, v75\n\t"
        "v_mfma_f32_32x32x16_bf16 v[0:15], v[148:151], v[64:67], v[0:15]\n\t"
        "v_exp_f32 v77, v77\n\t"
        "v_exp_f32 v78, v78\n\t"
        "v_exp_f32 v79, v79\n\t"
        "v_cvt_pk_bf16_f32 v74, v76, v77\n\t"
        "v_cvt_pk_bf16_f32 v75, v78, v79\n\t"
        "v_exp_f32 v80, v80\n\t"
        "v_exp_f32 v81, v81\n\t"
        "v_mfma_f32_32x32x16_bf16 v[0:15], v[152:155], v[72:75], v[0:15]\n\t"
        "v_exp_f32 v82, v82\n\t"
        "v_exp_f32 v83, v83\n\t"
        "v_cvt_pk_bf16_f32 v80, v80, v81\n\t"
        "v_exp_f32 v84, v84\n\t"
        "v_cvt_pk_bf16_f32 v81, v82, v83\n\t"
        "v_mfma_f32_32x32x8_bf16 v[64:79], v[144:145], v[116:117], 0\n\t"
        "v_exp_f32 v85, v85\n\t"
        "v_exp_f32 v86, v86\n\t"
        "v_exp_f32 v87, v87\n\t"
        "v_cvt_pk_bf16_f32 v82, v84, v85\n\t"
        "v_exp_f32 v88, v88\n\t"
        "v_cvt_pk_bf16_f32 v83, v86, v87\n\t"
        "v_mfma_f32_32x32x16_bf16 v[64:79], v[128:131], v[104:107], v[64:79]\n\t"
        "buffer_load_dwordx2 v[146:147], %[kb], %[srd], %[sk] offen offset:1024 sc1\n\t"
        "v_exp_f32 v89, v89\n\t"
        "v_exp_f32 v90, v90\n\t"
        "v_exp_f32 v91, v91\n\t"
        "v_cvt_pk_bf16_f32 v88, v88, v89\n\t"
        "v_exp_f32 v92, v92\n\t"
        "v_cvt_pk_bf16_f32 v89, v90, v91\n\t"
        "v_mfma_f32_32x32x16_bf16 v[16:31], v[148:151], v[80:83], v[16:31]\n\t"
        "v_exp_f32 v93, v93\n\t"
        "v_exp_f32 v94, v94\n\t"
        "v_exp_f32 v95, v95\n\t"
        "v_cvt_pk_bf16_f32 v90, v92, v93\n\t"
        "v_cvt_pk_bf16_f32 v91, v94, v95\n\t"
        "v_exp_f32 v64, v64\n\t"
        "v_exp_f32 v65, v65\n\t"
        "v_mfma_f32_32x32x16_bf16 v[16:31], v[152:155], v[88:91], v[16:31]\n\t"
        "v_exp_f32 v66, v66\n\t"
        "v_exp_f32 v67, v67\n\t"
        "v_cvt_pk_bf16_f32 v64, v64, v65\n\t"
        "v_exp_f32 v68, v68\n\t"
        "v_cvt_pk_bf16_f32 v65, v66, v67\n\t"
        "v_mfma_f32_32x32x8_bf16 v[80:95], v[144:145], v[118:119], 0\n\t"
        "v_exp_f32 v69, v69\n\t"
        "v_exp_f32 v70, v70\n\t"
        "v_exp_f32 v71, v71\n\t"
        "v_cvt_pk_bf16_f32 v66, v68, v69\n\t"
        "v_exp_f32 v72, v72\n\t"
        "v_cvt_pk_bf16_f32 v67, v70, v71\n\t"
        "v_mfma_f32_32x32x16_bf16 v[80:95], v[128:131], v[108:111], v[80:95]\n\t"
        "buffer_load_dwordx4 v[140:143], %[ka], %[srd], %[sk] offen offset:1536 sc1\n\t"
        "v_exp_f32 v73, v73\n\t"
        "v_exp_f32 v74, v74\n\t"
        "v_exp_f32 v75, v75\n\t"
        "v_cvt_pk_bf16_f32 v72, v72, v73\n\t"
        "v_exp_f32 v76, v76\n\t"
        "v_cvt_pk_bf16_f32 v73, v74, v75\n\t"
        "v_mfma_f32_32x32x16_bf16 v[32:47], v[148:151], v[64:67], v[32:47]\n\t"
        "v_exp_f32 v77, v77\n\t"
        "v_exp_f32 v78, v78\n\t"
        "v_exp_f32 v79, v79\n\t"
        "v_cvt_pk_bf16_f32 v74, v76, v77\n\t"
        "v_cvt_pk_bf16_f32 v75, v78, v79\n\t"
        "v_exp_f32 v80, v80\n\t"
        "v_exp_f32 v81, v81\n\t"
        "v_mfma_f32_32x32x16_bf16 v[32:47], v[152:155], v[72:75], v[32:47]\n\t"
        "v_exp_f32 v82, v82\n\t"
        "v_exp_f32 v83, v83\n\t"
        "v_cvt_pk_bf16_f32 v80, v80, v81\n\t"
        "v_exp_f32 v84, v84\n\t"
        "s_waitcnt vmcnt(5)\n\t"
        "v_cvt_pk_bf16_f32 v81, v82, v83\n\t"
        "v_mfma_f32_32x32x8_bf16 v[64:79], v[144:145], v[120:121], 0\n\t"
        "v_exp_f32 v85, v85\n\t"
        "v_exp_f32 v86, v86\n\t"
        "v_exp_f32 v87, v87\n\t"
        "v_cvt_pk_bf16_f32 v82, v84, v85\n\t"
        "v_exp_f32 v88, v88\n\t"
        "v_cvt_pk_bf16_f32 v83, v86, v87\n\t"
        "v_mfma_f32_32x32x16_bf16 v[64:79], v[132:135], v[96:99], v[64:79]\n\t"
        "v_exp_f32 v89, v89\n\t"
        "v_exp_f32 v90, v90\n\t"
        "v_exp_f32 v91, v91\n\t"
        "v_cvt_pk_bf16_f32 v88, v88, v89\n\t"
        "v_exp_f32 v92, v92\n\t"
        "v_cvt_pk_bf16_f32 v89, v90, v91\n\t"
        "v_mfma_f32_32x32x16_bf16 v[48:63], v[148:151], v[80:83], v[48:63]\n\t"
        "v_exp_f32 v93, v93\n\t"
        "v_exp_f32 v94, v94\n\t"
        "v_exp_f32 v95, v95\n\t"
        "v_cvt_pk_bf16_f32 v90, v92, v93\n\t"
        "v_cvt_pk_bf16_f32 v91, v94, v95\n\t"
        "5:\n\t"
        "v_exp_f32 v64, v64\n\t"
        "v_exp_f32 v65, v65\n\t"
        "v_mfma_f32_32x32x16_bf16 v[48:63], v[152:155], v[88:91], v[48:63]\n\t"
        "v_exp_f32 v66, v66\n\t"
        "v_exp_f32 v67, v67\n\t"
        "v_cvt_pk_bf16_f32 v64, v64, v65\n\t"
        "v_exp_f32 v68, v68\n\t"
        "v_cvt_pk_bf16_f32 v65, v66, v67\n\t"
        "v_mfma_f32_32x32x8_bf16 v[80:95], v[144:145], v[122:123], 0\n\t"
        "v_exp_f32 v69, v69\n\t"
        "v_exp_f32 v70, v70\n\t"
        "v_exp_f32 v71, v71\n\t"
        "v_cvt_pk_bf16_f32 v66, v68, v69\n\t"
        "v_exp_f32 v72, v72\n\t"
        "v_cvt_pk_bf16_f32 v67, v70, v71\n\t"
        "v_mfma_f32_32x32x16_bf16 v[80:95], v[132:135], v[100:103], v[80:95]\n\t"
        "buffer_load_dwordx4 v[172:175], %[vo], %[srd], %[sv] offen offset:0 sc1\n\t"
        "v_exp_f32 v73, v73\n\t"
        "v_exp_f32 v74, v74\n\t"
        "buffer_load_dwordx4 v[176:179], %[vo], %[srd], %[sv] offen offset:2176 sc1\n\t"
        "v_exp_f32 v75, v75\n\t"
        "v_cvt_pk_bf16_f32 v72, v72, v73\n\t"
        "v_exp_f32 v76, v76\n\t"
        "s_add_u32 %[sv], %[sv], 8704\n\t"
        "v_cvt_pk_bf16_f32 v73, v74, v75\n\t"
        "v_mfma_f32_32x32x16_bf16 v[0:15], v[156:159], v[64:67], v[0:15]\n\t"
        "v_exp_f32 v77, v77\n\t"
        "v_exp_f32 v78, v78\n\t"
        "v_exp_f32 v79, v79\n\t"
        "v_cvt_pk_bf16_f32 v74, v76, v77\n\t"
        "v_cvt_pk_bf16_f32 v75, v78, v79\n\t"
        "v_exp_f32 v80, v80\n\t"
        "v_exp_f32 v81, v81\n\t"
        "v_mfma_f32_32x32x16_bf16 v[0:15], v[160:163], v[72:75], v[0:15]\n\t"
        "v_exp_f32 v82, v82\n\t"
        "v_exp_f32 v83, v83\n\t"
        "v_cvt_pk_bf16_f32 v80, v80, v81\n\t"
        "v_exp_f32 v84, v84\n\t"
        "v_cvt_pk_bf16_f32 v81, v82, v83\n\t"
        "v_mfma_f32_32x32x8_bf16 v[64:79], v[144:145], v[124:125], 0\n\t"
        "v_exp_f32 v85, v85\n\t"
        "v_exp_f32 v86, v86\n\t"
        "v_exp_f32 v87, v87\n\t"
        "v_cvt_pk_bf16_f32 v82, v84, v85\n\t"
        "v_exp_f32 v88, v88\n\t"
        "v_cvt_pk_bf16_f32 v83, v86, v87\n\t"
        "v_mfma_f32_32x32x16_bf16 v[64:79], v[132:135], v[104:107], v[64:79]\n\t"
        "v_exp_f32 v89, v89\n\t"
        "v_exp_f32 v90, v90\n\t"
        "v_exp_f32 v91, v91\n\t"
        "v_cvt_pk_bf16_f32 v88, v88, v89\n\t"
        "v_exp_f32 v92, v92\n\t"
        "v_cvt_pk_bf16_f32 v89, v90, v91\n\t"
        "v_mfma_f32_32x32x16_bf16 v[16:31], v[156:159], v[80:83], v[16:31]\n\t"
        "v_exp_f32 v93, v93\n\t"
        "v_exp_f32 v94, v94\n\t"
        "v_exp_f32 v95, v95\n\t"
        "v_cvt_pk_bf16_f32 v90, v92, v93\n\t"
        "v_cvt_pk_bf16_f32 v91, v94, v95\n\t"
        "v_exp_f32 v64, v64\n\t"
        "v_exp_f32 v65, v65\n\t"
        "v_mfma_f32_32x32x16_bf16 v[16:31], v[160:163], v[88:91], v[16:31]\n\t"
        "v_exp_f32 v66, v66\n\t"
        "v_exp_f32 v67, v67\n\t"
        "v_cvt_pk_bf16_f32 v64, v64, v65\n\t"
        "v_exp_f32 v68, v68\n\t"
        "v_cvt_pk_bf16_f32 v65, v66, v67\n\t"
        "v_mfma_f32_32x32x8_bf16 v[80:95], v[144:145], v[126:127], 0\n\t"
        "v_exp_f32 v69, v69\n\t"
        "v_exp_f32 v70, v70\n\t"
        "v_exp_f32 v71, v71\n\t"
        "v_cvt_pk_bf16_f32 v66, v68, v69\n\t"
        "v_exp_f32 v72, v72\n\t"
        "v_cvt_pk_bf16_f32 v67, v70, v71\n\t"
        "v_mfma_f32_32x32x16_bf16 v[80:95], v[132:135], v[108:111], v[80:95]\n\t"
        "buffer_load_dwordx4 v[128:131], %[ka], %[srd], %[sk] offen offset:2048 sc1\n\t"
        "v_exp_f32 v73, v73\n\t"
        "v_exp_f32 v74, v74\n\t"
        "v_exp_f32 v75, v75\n\t"
        "v_cvt_pk_bf16_f32 v72, v72, v73\n\t"
        "v_exp_f32 v76, v76\n\t"
        "v_cvt_pk_bf16_f32 v73, v74, v75\n\t"
        "v_mfma_f32_32x32x16_bf16 v[32:47], v[156:159], v[64:67], v[32:47]\n\t"
        "v_exp_f32 v77, v77\n\t"
        "v_exp_f32 v78, v78\n\t"
        "v_exp_f32 v79, v79\n\t"
        "v_cvt_pk_bf16_f32 v74, v76, v77\n\t"
        "v_cvt_pk_bf16_f32 v75, v78, v79\n\t"
        "v_exp_f32 v80, v80\n\t"
        "v_exp_f32 v81, v81\n\t"
        "v_mfma_f32_32x32x16_bf16 v[32:47], v[160:163], v[72:75], v[32:47]\n\t"
        "v_exp_f32 v82, v82\n\t"
        "v_exp_f32 v83, v83\n\t"
        "v_cvt_pk_bf16_f32 v80, v80, v81\n\t"
        "v_exp_f32 v84, v84\n\t"
        "s_waitcnt vmcnt(4)\n\t"
        "v_cvt_pk_bf16_f32 v81, v82, v83\n\t"
        "v_mfma_f32_32x32x8_bf16 v[64:79], v[146:147], v[112:113], 0\n\t"
        "v_exp_f32 v85, v85\n\t"
        "v_exp_f32 v86, v86\n\t"
        "v_exp_f32 v87, v87\n\t"
        "v_cvt_pk_bf16_f32 v82, v84, v85\n\t"
        "v_exp_f32 v88, v88\n\t"
        "v_cvt_pk_bf16_f32 v83, v86, v87\n\t"
        "v_mfma_f32_32x32x16_bf16 v[64:79], v[136:139], v[96:99], v[64:79]\n\t"
        "v_exp_f32 v89, v89\n\t"
        "v_exp_f32 v90, v90\n\t"
        "v_exp_f32 v91, v91\n\t"
        "v_cvt_pk_bf16_f32 v88, v88, v89\n\t"
        "v_exp_f32 v92, v92\n\t"
        "v_cvt_pk_bf16_f32 v89, v90, v91\n\t"
        "v_mfma_f32_32x32x16_bf16 v[48:63], v[156:159], v[80:83], v[48:63]\n\t"
        "v_exp_f32 v93, v93\n\t"
        "v_exp_f32 v94, v94\n\t"
        "v_exp_f32 v95, v95\n\t"
        "v_cvt_pk_bf16_f32 v90, v92, v93\n\t"
        "v_cvt_pk_bf16_f32 v91, v94, v95\n\t"
        "v_exp_f32 v64, v64\n\t"
        "v_exp_f32 v65, v65\n\t"
        "v_mfma_f32_32x32x16_bf16 v[48:63], v[160:163], v[88:91], v[48:63]\n\t"
        "v_exp_f32 v66, v66\n\t"
        "v_exp_f32 v67, v67\n\t"
        "v_cvt_pk_bf16_f32 v64, v64, v65\n\t"
        "v_exp_f32 v68, v68\n\t"
        "v_cvt_pk_bf16_f32 v65, v66, v67\n\t"
        "v_mfma_f32_32x32x8_bf16 v[80:95], v[146:147], v[114:115], 0\n\t"
        "v_exp_f32 v69, v69\n\t"
        "v_exp_f32 v70, v70\n\t"
        "v_exp_f32 v71, v71\n\t"
        "v_cvt_pk_bf16_f32 v66, v68, v69\n\t"
        "v_exp_f32 v72, v72\n\t"
        "v_cvt_pk_bf16_f32 v67, v70, v71\n\t"
        "v_mfma_f32_32x32x16_bf16 v[80:95], v[136:139], v[100:103], v[80:95]\n\t"
        "buffer_load_dwordx4 v[148:151], %[vo], %[srd], %[sv] offen offset:0 sc1\n\t"
        "v_exp_f32 v73, v73\n\t"
        "v_exp_f32 v74, v74\n\t"
        "buffer_load_dwordx4 v[152:155], %[vo], %[srd], %[sv] offen offset:2176 sc1\n\t"
        "v_exp_f32 v75, v75\n\t"
        "v_cvt_pk_bf16_f32 v72, v72, v73\n\t"
        "v_exp_f32 v76, v76\n\t"
        "s_add_u32 %[sv], %[sv], 8704\n\t"
        "v_cvt_pk_bf16_f32 v73, v74, v75\n\t"
        "v_mfma_f32_32x32x16_bf16 v[0:15], v[164:167], v[64:67], v[0:15]\n\t"
        "v_exp_f32 v77, v77\n\t"
        "v_exp_f32 v78, v78\n\t"
        "v_exp_f32 v79, v79\n\t"
        "v_cvt_pk_bf16_f32 v74, v76, v77\n\t"
        "v_cvt_pk_bf16_f32 v75, v78, v79\n\t"
        "v_exp_f32 v80, v80\n\t"
        "v_exp_f32 v81, v81\n\t"
        "v_mfma_f32_32x32x16_bf16 v[0:15], v[168:171], v[72:75], v[0:15]\n\t"
        "v_exp_f32 v82, v82\n\t"
        "v_exp_f32 v83, v83\n\t"
        "v_cvt_pk_bf16_f32 v80, v80, v81\n\t"
        "v_exp_f32 v84, v84\n\t"
        "v_cvt_pk_bf16_f32 v81, v82, v83\n\t"
        "v_mfma_f32_32x32x8_bf16 v[64:79], v[146:147], v[116:117], 0\n\t"
        "v_exp_f32 v85, v85\n\t"
        "v_exp_f32 v86, v86\n\t"
        "v_exp_f32 v87, v87\n\t"
        "v_cvt_pk_bf16_f32 v82, v84, v85\n\t"
        "v_exp_f32 v88, v88\n\t"
        "v_cvt_pk_bf16_f32 v83, v86, v87\n\t"
        "v_mfma_f32_32x32x16_bf16 v[64:79], v[136:139], v[104:107], v[64:79]\n\t"
        "buffer_load_dwordx2 v[144:145], %[kb], %[srd], %[sk] offen offset:2048 sc1\n\t"
        "v_exp_f32 v89, v89\n\t"
        "v_exp_f32 v90, v90\n\t"
        "v_exp_f32 v91, v91\n\t"
        "v_cvt_pk_bf16_f32 v88, v88, v89\n\t"
        "v_exp_f32 v92, v92\n\t"
        "v_cvt_pk_bf16_f32 v89, v90, v91\n\t"
        "v_mfma_f32_32x32x16_bf16 v[16:31], v[164:167], v[80:83], v[16:31]\n\t"
        "v_exp_f32 v93, v93\n\t"
        "v_exp_f32 v94, v94\n\t"
        "v_exp_f32 v95, v95\n\t"
        "v_cvt_pk_bf16_f32 v90, v92, v93\n\t"
        "v_cvt_pk_bf16_f32 v91, v94, v95\n\t"
        "v_exp_f32 v64, v64\n\t"
        "v_exp_f32 v65, v65\n\t"
        "v_mfma_f32_32x32x16_bf16 v[16:31], v[168:171], v[88:91], v[16:31]\n\t"
        "v_exp_f32 v66, v66\n\t"
        "v_exp_f32 v67, v67\n\t"
        "v_cvt_pk_bf16_f32 v64, v64, v65\n\t"
        "v_exp_f32 v68, v68\n\t"
        "v_cvt_pk_bf16_f32 v65, v66, v67\n\t"
        "v_mfma_f32_32x32x8_bf16 v[80:95], v[146:147], v[118:119], 0\n\t"
        "v_exp_f32 v69, v69\n\t"
        "v_exp_f32 v70, v70\n\t"
        "v_exp_f32 v71, v71\n\t"
        "v_cvt_pk_bf16_f32 v66, v68, v69\n\t"
        "v_exp_f32 v72, v72\n\t"
        "v_cvt_pk_bf16_f32 v67, v70, v71\n\t"
        "v_mfma_f32_32x32x16_bf16 v[80:95], v[136:139], v[108:111], v[80:95]\n\t"
        "buffer_load_dwordx4 v[132:135], %[ka], %[srd], %[sk] offen offset:2560 sc1\n\t"
        "v_exp_f32 v73, v73\n\t"
        "v_exp_f32 v74, v74\n\t"
        "v_exp_f32 v75, v75\n\t"
        "v_cvt_pk_bf16_f32 v72, v72, v73\n\t"
        "v_exp_f32 v76, v76\n\t"
        "v_cvt_pk_bf16_f32 v73, v74, v75\n\t"
        "v_mfma_f32_32x32x16_bf16 v[32:47], v[164:167], v[64:67], v[32:47]\n\t"
        "v_exp_f32 v77, v77\n\t"
        "v_exp_f32 v78, v78\n\t"
        "v_exp_f32 v79, v79\n\t"
        "v_cvt_pk_bf16_f32 v74, v76, v77\n\t"
        "v_cvt_pk_bf16_f32 v75, v78, v79\n\t"
        "v_exp_f32 v80, v80\n\t"
        "v_exp_f32 v81, v81\n\t"
        "v_mfma_f32_32x32x16_bf16 v[32:47], v[168:171], v[72:75], v[32:47]\n\t"
        "v_exp_f32 v82, v82\n\t"
        "v_exp_f32 v83, v83\n\t"
        "v_cvt_pk_bf16_f32 v80, v80, v81\n\t"
        "v_exp_f32 v84, v84\n\t"
        "s_waitcnt vmcnt(5)\n\t"
        "v_cvt_pk_bf16_f32 v81, v82, v83\n\t"
        "v_mfma_f32_32x32x8_bf16 v[64:79], v[146:147], v[120:121], 0\n\t"
        "v_exp_f32 v85, v85\n\t"
        "v_exp_f32 v86, v86\n\t"
        "v_exp_f32 v87, v87\n\t"
        "v_cvt_pk_bf16_f32 v82, v84, v85\n\t"
        "v_exp_f32 v88, v88\n\t"
        "v_cvt_pk_bf16_f32 v83, v86, v87\n\t"
        "v_mfma_f32_32x32x16_bf16 v[64:79], v[140:143], v[96:99], v[64:79]\n\t"
        "v_exp_f32 v89, v89\n\t"
        "v_exp_f32 v90, v90\n\t"
        "v_exp_f32 v91, v91\n\t"
        "v_cvt_pk_bf16_f32 v88, v88, v89\n\t"
        "v_exp_f32 v92, v92\n\t"
        "v_cvt_pk_bf16_f32 v89, v90, v91\n\t"
        "v_mfma_f32_32x32x16_bf16 v[48:63], v[164:167], v[80:83], v[48:63]\n\t"
        "v_exp_f32 v93, v93\n\t"
        "v_exp_f32 v94, v94\n\t"
        "v_exp_f32 v95, v95\n\t"
        "v_cvt_pk_bf16_f32 v90, v92, v93\n\t"
        "v_cvt_pk_bf16_f32 v91, v94, v95\n\t"
        "v_exp_f32 v64, v64\n\t"
        "v_exp_f32 v65, v65\n\t"
        "v_mfma_f32_32x32x16_bf16 v[48:63], v[168:171], v[88:91], v[48:63]\n\t"
        "v_exp_f32 v66, v66\n\t"
        "v_exp_f32 v67, v67\n\t"
        "v_cvt_pk_bf16_f32 v64, v64, v65\n\t"
        "v_exp_f32 v68, v68\n\t"
        "v_cvt_pk_bf16_f32 v65, v66, v67\n\t"
        "v_mfma_f32_32x32x8_bf16 v[80:95], v[146:147], v[122:123], 0\n\t"
        "v_exp_f32 v69, v69\n\t"
        "v_exp_f32 v70, v70\n\t"
        "v_exp_f32 v71, v71\n\t"
        "v_cvt_pk_bf16_f32 v66, v68, v69\n\t"
        "v_exp_f32 v72, v72\n\t"
        "v_cvt_pk_bf16_f32 v67, v70, v71\n\t"
        "v_mfma_f32_32x32x16_bf16 v[80:95], v[140:143], v[100:103], v[80:95]\n\t"
        "buffer_load_dwordx4 v[156:159], %[vo], %[srd], %[sv] offen offset:0 sc1\n\t"
        "v_exp_f32 v73, v73\n\t"
        "v_exp_f32 v74, v74\n\t"
        "buffer_load_dwordx4 v[160:163], %[vo], %[srd], %[sv] offen offset:2176 sc1\n\t"
        "v_exp_f32 v75, v75\n\t"
        "v_cvt_pk_bf16_f32 v72, v72, v73\n\t"
        "v_exp_f32 v76, v76\n\t"
        "s_add_u32 %[sv], %[sv], 8704\n\t"
        "v_cvt_pk_bf16_f32 v73, v74, v75\n\t"
        "v_mfma_f32_32x32x16_bf16 v[0:15], v[172:175], v[64:67], v[0:15]\n\t"
        "v_exp_f32 v77, v77\n\t"
        "v_exp_f32 v78, v78\n\t"
        "v_exp_f32 v79, v79\n\t"
        "v_cvt_pk_bf16_f32 v74, v76, v77\n\t"
        "v_cvt_pk_bf16_f32 v75, v78, v79\n\t"
        "v_exp_f32 v80, v80\n\t"
        "v_exp_f32 v81, v81\n\t"
        "v_mfma_f32_32x32x16_bf16 v[0:15], v[176:179], v[72:75], v[0:15]\n\t"
        "v_exp_f32 v82, v82\n\t"
        "v_exp_f32 v83, v83\n\t"
        "v_cvt_pk_bf16_f32 v80, v80, v81\n\t"
        "v_exp_f32 v84, v84\n\t"
        "v_cvt_pk_bf16_f32 v81, v82, v83\n\t"
        "v_mfma_f32_32x32x8_bf16 v[64:79], v[146:147], v[124:125], 0\n\t"
        "v_exp_f32 v85, v85\n\t"
        "v_exp_f32 v86, v86\n\t"
        "v_exp_f32 v87, v87\n\t"
        "v_cvt_pk_bf16_f32 v82, v84, v85\n\t"
        "v_exp_f32 v88, v88\n\t"
        "v_cvt_pk_bf16_f32 v83, v86, v87\n\t"
        "v_mfma_f32_32x32x16_bf16 v[64:79], v[140:143], v[104:107], v[64:79]\n\t"
        "v_exp_f32 v89, v89\n\t"
        "v_exp_f32 v90, v90\n\t"
        "v_exp_f32 v91, v91\n\t"
        "v_cvt_pk_bf16_f32 v88, v88, v89\n\t"
        "v_exp_f32 v92, v92\n\t"
        "v_cvt_pk_bf16_f32 v89, v90, v91\n\t"
        "v_mfma_f32_32x32x16_bf16 v[16:31], v[172:175], v[80:83], v[16:31]\n\t"
        "v_exp_f32 v93, v93\n\t"
        "v_exp_f32 v94, v94\n\t"
        "v_exp_f32 v95, v95\n\t"
        "v_cvt_pk_bf16_f32 v90, v92, v93\n\t"
        "v_cvt_pk_bf16_f32 v91, v94, v95\n\t"
        "v_exp_f32 v64, v64\n\t"
        "v_exp_f32 v65, v65\n\t"
        "v_mfma_f32_32x32x16_bf16 v[16:31], v[176:179], v[88:91], v[16:31]\n\t"
        "v_exp_f32 v66, v66\n\t"
        "v_exp_f32 v67, v67\n\t"
        "v_cvt_pk_bf16_f32 v64, v64, v65\n\t"
        "v_exp_f32 v68, v68\n\t"
        "v_cvt_pk_bf16_f32 v65, v66, v67\n\t"
        "v_mfma_f32_32x32x8_bf16 v[80:95], v[146:147], v[126:127], 0\n\t"
        "v_exp_f32 v69, v69\n\t"
        "v_exp_f32 v70, v70\n\t"
        "v_exp_f32 v71, v71\n\t"
        "v_cvt_pk_bf16_f32 v66, v68, v69\n\t"
        "v_exp_f32 v72, v72\n\t"
        "v_cvt_pk_bf16_f32 v67, v70, v71\n\t"
        "v_mfma_f32_32x32x16_bf16 v[80:95], v[140:143], v[108:111], v[80:95]\n\t"
        "buffer_load_dwordx4 v[136:139], %[ka], %[srd], %[sk] offen offset:3072 sc1\n\t"
        "v_exp_f32 v73, v73\n\t"
        "v_exp_f32 v74, v74\n\t"
        "v_exp_f32 v75, v75\n\t"
        "v_cvt_pk_bf16_f32 v72, v72, v73\n\t"
        "v_exp_f32 v76, v76\n\t"
        "v_cvt_pk_bf16_f32 v73, v74, v75\n\t"
        "v_mfma_f32_32x32x16_bf16 v[32:47], v[172:175], v[64:67], v[32:47]\n\t"
        "v_exp_f32 v77, v77\n\t"
        "v_exp_f32 v78, v78\n\t"
        "v_exp_f32 v79, v79\n\t"
        "v_cvt_pk_bf16_f32 v74, v76, v77\n\t"
        "v_cvt_pk_bf16_f32 v75, v78, v79\n\t"
        "v_exp_f32 v80, v80\n\t"
        "v_exp_f32 v81, v81\n\t"
        "v_mfma_f32_32x32x16_bf16 v[32:47], v[176:179], v[72:75], v[32:47]\n\t"
        "v_exp_f32 v82, v82\n\t"
        "v_exp_f32 v83, v83\n\t"
        "v_cvt_pk_bf16_f32 v80, v80, v81\n\t"
        "v_exp_f32 v84, v84\n\t"
        "s_waitcnt vmcnt(4)\n\t"
        "v_cvt_pk_bf16_f32 v81, v82, v83\n\t"
        "v_mfma_f32_32x32x8_bf16 v[64:79], v[144:145], v[112:113], 0\n\t"
        "v_exp_f32 v85, v85\n\t"
        "v_exp_f32 v86, v86\n\t"
        "v_exp_f32 v87, v87\n\t"
        "v_cvt_pk_bf16_f32 v82, v84, v85\n\t"
        "v_exp_f32 v88, v88\n\t"
        "v_cvt_pk_bf16_f32 v83, v86, v87\n\t"
        "v_mfma_f32_32x32x16_bf16 v[64:79], v[128:131], v[96:99], v[64:79]\n\t"
        "v_exp_f32 v89, v89\n\t"
        "v_exp_f32 v90, v90\n\t"
        "v_exp_f32 v91, v91\n\t"
        "v_cvt_pk_bf16_f32 v88, v88, v89\n\t"
        "v_exp_f32 v92, v92\n\t"
        "v_cvt_pk_bf16_f32 v89, v90, v91\n\t"
        "v_mfma_f32_32x32x16_bf16 v[48:63], v[172:175], v[80:83], v[48:63]\n\t"
        "s_add_u32 %[sk], %[sk], 2048\n\t"
        "v_exp_f32 v93, v93\n\t"
        "v_exp_f32 v94, v94\n\t"
        "s_sub_u32 %[cnt], %[cnt], 1\n\t"
        "v_exp_f32 v95, v95\n\t"
        "v_cvt_pk_bf16_f32 v90, v92, v93\n\t"
        "s_cmp_lg_u32 %[cnt], 0\n\t"
        "v_cvt_pk_bf16_f32 v91, v94, v95\n\t"
        "s_cbranch_scc1 1b\n\t"
        "v_exp_f32 v64, v64\n\t"
        "v_exp_f32 v65, v65\n\t"
        "v_mfma_f32_32x32x16_bf16 v[48:63], v[176:179], v[88:91], v[48:63]\n\t"
        "v_exp_f32 v66, v66\n\t"
        "v_exp_f32 v67, v67\n\t"
        "v_cvt_pk_bf16_f32 v64, v64, v65\n\t"
        "v_exp_f32 v68, v68\n\t"
        "v_cvt_pk_bf16_f32 v65, v66, v67\n\t"
        "v_mfma_f32_32x32x8_bf16 v[80:95], v[144:145], v[114:115], 0\n\t"
        "v_exp_f32 v69, v69\n\t"
        "v_exp_f32 v70, v70\n\t"
        "v_exp_f32 v71, v71\n\t"
        "v_cvt_pk_bf16_f32 v66, v68, v69\n\t"
        "v_exp_f32 v72, v72\n\t"
        "v_cvt_pk_bf16_f32 v67, v70, v71\n\t"
        "v_mfma_f32_32x32x16_bf16 v[80:95], v[128:131], v[100:103], v[80:95]\n\t"
        "buffer_load_dwordx4 v[164:167], %[vo], %[srd], %[sv] offen offset:0 sc1\n\t"
        "v_exp_f32 v73, v73\n\t"
        "v_exp_f32 v74, v74\n\t"
        "buffer_load_dwordx4 v[168:171], %[vo], %[srd], %[sv] offen offset:2176 sc1\n\t"
        "v_exp_f32 v75, v75\n\t"
        "v_cvt_pk_bf16_f32 v72, v72, v73\n\t"
        "v_exp_f32 v76, v76\n\t"
        "s_add_u32 %[sv], %[sv], 8704\n\t"
        "v_cvt_pk_bf16_f32 v73, v74, v75\n\t"
        "v_mfma_f32_32x32x16_bf16 v[0:15], v[148:151], v[64:67], v[0:15]\n\t"
        "v_exp_f32 v77, v77\n\t"
        "v_exp_f32 v78, v78\n\t"
        "v_exp_f32 v79, v79\n\t"
        "v_cvt_pk_bf16_f32 v74, v76, v77\n\t"
        "v_cvt_pk_bf16_f32 v75, v78, v79\n\t"
        "v_exp_f32 v80, v80\n\t"
        "v_exp_f32 v81, v81\n\t"
        "v_mfma_f32_32x32x16_bf16 v[0:15], v[152:155], v[72:75], v[0:15]\n\t"
        "v_exp_f32 v82, v82\n\t"
        "v_exp_f32 v83, v83\n\t"
        "v_cvt_pk_bf16_f32 v80, v80, v81\n\t"
        "v_exp_f32 v84, v84\n\t"
        "v_cvt_pk_bf16_f32 v81, v82, v83\n\t"
        "v_mfma_f32_32x32x8_bf16 v[64:79], v[144:145], v[116:117], 0\n\t"
        "v_exp_f32 v85, v85\n\t"
        "v_exp_f32 v86, v86\n\t"
        "v_exp_f32 v87, v87\n\t"
        "v_cvt_pk_bf16_f32 v82, v84, v85\n\t"
        "v_exp_f32 v88, v88\n\t"
        "v_cvt_pk_bf16_f32 v83, v86, v87\n\t"
        "v_mfma_f32_32x32x16_bf16 v[64:79], v[128:131], v[104:107], v[64:79]\n\t"
        "buffer_load_dwordx2 v[146:147], %[kb], %[srd], %[sk] offen offset:1024 sc1\n\t"
        "v_exp_f32 v89, v89\n\t"
        "v_exp_f32 v90, v90\n\t"
        "v_exp_f32 v91, v91\n\t"
        "v_cvt_pk_bf16_f32 v88, v88, v89\n\t"
        "v_exp_f32 v92, v92\n\t"
        "v_cvt_pk_bf16_f32 v89, v90, v91\n\t"
        "v_mfma_f32_32x32x16_bf16 v[16:31], v[148:151], v[80:83], v[16:31]\n\t"
        "v_exp_f32 v93, v93\n\t"
        "v_exp_f32 v94, v94\n\t"
        "v_exp_f32 v95, v95\n\t"
        "v_cvt_pk_bf16_f32 v90, v92, v93\n\t"
        "v_cvt_pk_bf16_f32 v91, v94, v95\n\t"
        "v_exp_f32 v64, v64\n\t"
        "v_exp_f32 v65, v65\n\t"
        "v_mfma_f32_32x32x16_bf16 v[16:31], v[152:155], v[88:91], v[16:31]\n\t"
        "v_exp_f32 v66, v66\n\t"
        "v_exp_f32 v67, v67\n\t"
        "v_cvt_pk_bf16_f32 v64, v64, v65\n\t"
        "v_exp_f32 v68, v68\n\t"
        "v_cvt_pk_bf16_f32 v65, v66, v67\n\t"
        "v_mfma_f32_32x32x8_bf16 v[80:95], v[144:145], v[118:119], 0\n\t"
        "v_exp_f32 v69, v69\n\t"
        "v_exp_f32 v70, v70\n\t"
        "v_exp_f32 v71, v71\n\t"
        "v_cvt_pk_bf16_f32 v66, v68, v69\n\t"
        "v_exp_f32 v72, v72\n\t"
        "v_cvt_pk_bf16_f32 v67, v70, v71\n\t"
        "v_mfma_f32_32x32x16_bf16 v[80:95], v[128:131], v[108:111], v[80:95]\n\t"
        "buffer_load_dwordx4 v[140:143], %[ka], %[srd], %[sk] offen offset:1536 sc1\n\t"
        "v_exp_f32 v73, v73\n\t"
        "v_exp_f32 v74, v74\n\t"
        "v_exp_f32 v75, v75\n\t"
        "v_cvt_pk_bf16_f32 v72, v72, v73\n\t"
        "v_exp_f32 v76, v76\n\t"
        "v_cvt_pk_bf16_f32 v73, v74, v75\n\t"
        "v_mfma_f32_32x32x16_bf16 v[32:47], v[148:151], v[64:67], v[32:47]\n\t"
        "v_exp_f32 v77, v77\n\t"
        "v_exp_f32 v78, v78\n\t"
        "v_exp_f32 v79, v79\n\t"
        "v_cvt_pk_bf16_f32 v74, v76, v77\n\t"
        "v_cvt_pk_bf16_f32 v75, v78, v79\n\t"
        "v_exp_f32 v80, v80\n\t"
        "v_exp_f32 v81, v81\n\t"
        "v_mfma_f32_32x32x16_bf16 v[32:47], v[152:155], v[72:75], v[32:47]\n\t"
        "v_exp_f32 v82, v82\n\t"
        "v_exp_f32 v83, v83\n\t"
        "v_cvt_pk_bf16_f32 v80, v80, v81\n\t"
        "v_exp_f32 v84, v84\n\t"
        "s_waitcnt vmcnt(5)\n\t"
        "v_cvt_pk_bf16_f32 v81, v82, v83\n\t"
        "v_mfma_f32_32x32x8_bf16 v[64:79], v[144:145], v[120:121], 0\n\t"
        "v_exp_f32 v85, v85\n\t"
        "v_exp_f32 v86, v86\n\t"
        "v_exp_f32 v87, v87\n\t"
        "v_cvt_pk_bf16_f32 v82, v84, v85\n\t"
        "v_exp_f32 v88, v88\n\t"
        "v_cvt_pk_bf16_f32 v83, v86, v87\n\t"
        "v_mfma_f32_32x32x16_bf16 v[64:79], v[132:135], v[96:99], v[64:79]\n\t"
        "v_exp_f32 v89, v89\n\t"
        "v_exp_f32 v90, v90\n\t"
        "v_exp_f32 v91, v91\n\t"
        "v_cvt_pk_bf16_f32 v88, v88, v89\n\t"
        "v_exp_f32 v92, v92\n\t"
        "v_cvt_pk_bf16_f32 v89, v90, v91\n\t"
        "v_mfma_f32_32x32x16_bf16 v[48:63], v[148:151], v[80:83], v[48:63]\n\t"
        "v_exp_f32 v93, v93\n\t"
        "v_exp_f32 v94, v94\n\t"
        "v_exp_f32 v95, v95\n\t"
        "v_cvt_pk_bf16_f32 v90, v92, v93\n\t"
        "v_cvt_pk_bf16_f32 v91, v94, v95\n\t"
        "v_exp_f32 v64, v64\n\t"
        "v_exp_f32 v65, v65\n\t"
        "v_mfma_f32_32x32x16_bf16 v[48:63], v[152:155], v[88:91], v[48:63]\n\t"
        "v_exp_f32 v66, v66\n\t"
        "v_exp_f32 v67, v67\n\t"
        "v_cvt_pk_bf16_f32 v64, v64, v65\n\t"
        "v_exp_f32 v68, v68\n\t"
        "v_cvt_pk_bf16_f32 v65, v66, v67\n\t"
        "v_mfma_f32_32x32x8_bf16 v[80:95], v[144:145], v[122:123], 0\n\t"
        "v_exp_f32 v69, v69\n\t"
        "v_exp_f32 v70, v70\n\t"
        "v_exp_f32 v71, v71\n\t"
        "v_cvt_pk_bf16_f32 v66, v68, v69\n\t"
        "v_exp_f32 v72, v72\n\t"
        "v_cvt_pk_bf16_f32 v67, v70, v71\n\t"
        "v_mfma_f32_32x32x16_bf16 v[80:95], v[132:135], v[100:103], v[80:95]\n\t"
        "buffer_load_dwordx4 v[172:175], %[vo], %[srd], %[sv] offen offset:0 sc1\n\t"
        "v_exp_f32 v73, v73\n\t"
        "v_exp_f32 v74, v74\n\t"
        "buffer_load_dwordx4 v[176:179], %[vo], %[srd], %[sv] offen offset:2176 sc1\n\t"
        "v_exp_f32 v75, v75\n\t"
        "v_cvt_pk_bf16_f32 v72, v72, v73\n\t"
        "v_exp_f32 v76, v76\n\t"
        "s_add_u32 %[sv], %[sv], 8704\n\t"
        "v_cvt_pk_bf16_f32 v73, v74, v75\n\t"
        "v_mfma_f32_32x32x16_bf16 v[0:15], v[156:159], v[64:67], v[0:15]\n\t"
        "v_exp_f32 v77, v77\n\t"
        "v_exp_f32 v78, v78\n\t"
        "v_exp_f32 v79, v79\n\t"
        "v_cvt_pk_bf16_f32 v74, v76, v77\n\t"
        "v_cvt_pk_bf16_f32 v75, v78, v79\n\t"
        "v_exp_f32 v80, v80\n\t"
        "v_exp_f32 v81, v81\n\t"
        "v_mfma_f32_32x32x16_bf16 v[0:15], v[160:163], v[72:75], v[0:15]\n\t"
        "v_exp_f32 v82, v82\n\t"
        "v_exp_f32 v83, v83\n\t"
        "v_cvt_pk_bf16_f32 v80, v80, v81\n\t"
        "v_exp_f32 v84, v84\n\t"
        "v_cvt_pk_bf16_f32 v81, v82, v83\n\t"
        "v_mfma_f32_32x32x8_bf16 v[64:79], v[144:145], v[124:125], 0\n\t"
        "v_exp_f32 v85, v85\n\t"
        "v_exp_f32 v86, v86\n\t"
        "v_exp_f32 v87, v87\n\t"
        "v_cvt_pk_bf16_f32 v82, v84, v85\n\t"
        "v_exp_f32 v88, v88\n\t"
        "v_cvt_pk_bf16_f32 v83, v86, v87\n\t"
        "v_mfma_f32_32x32x16_bf16 v[64:79], v[132:135], v[104:107], v[64:79]\n\t"
        "v_exp_f32 v89, v89\n\t"
        "v_exp_f32 v90, v90\n\t"
        "v_exp_f32 v91, v91\n\t"
        "v_cvt_pk_bf16_f32 v88, v88, v89\n\t"
        "v_exp_f32 v92, v92\n\t"
        "v_cvt_pk_bf16_f32 v89, v90, v91\n\t"
        "v_mfma_f32_32x32x16_bf16 v[16:31], v[156:159], v[80:83], v[16:31]\n\t"
        "v_exp_f32 v93, v93\n\t"
        "v_exp_f32 v94, v94\n\t"
        "v_exp_f32 v95, v95\n\t"
        "v_cvt_pk_bf16_f32 v90, v92, v93\n\t"
        "v_cvt_pk_bf16_f32 v91, v94, v95\n\t"
        "v_exp_f32 v64, v64\n\t"
        "v_exp_f32 v65, v65\n\t"
        "v_mfma_f32_32x32x16_bf16 v[16:31], v[160:163], v[88:91], v[16:31]\n\t"
        "v_exp_f32 v66, v66\n\t"
        "v_exp_f32 v67, v67\n\t"
        "v_cvt_pk_bf16_f32 v64, v64, v65\n\t"
        "v_exp_f32 v68, v68\n\t"
        "v_cvt_pk_bf16_f32 v65, v66, v67\n\t"
        "v_mfma_f32_32x32x8_bf16 v[80:95], v[144:145], v[126:127], 0\n\t"
        "v_exp_f32 v69, v69\n\t"
        "v_exp_f32 v70, v70\n\t"
        "v_exp_f32 v71, v71\n\t"
        "v_cvt_pk_bf16_f32 v66, v68, v69\n\t"
        "v_exp_f32 v72, v72\n\t"
        "v_cvt_pk_bf16_f32 v67, v70, v71\n\t"
        "v_mfma_f32_32x32x16_bf16 v[80:95], v[132:135], v[108:111], v[80:95]\n\t"
        "v_exp_f32 v73, v73\n\t"
        "v_exp_f32 v74, v74\n\t"
        "v_exp_f32 v75, v75\n\t"
        "v_cvt_pk_bf16_f32 v72, v72, v73\n\t"
        "v_exp_f32 v76, v76\n\t"
        "v_cvt_pk_bf16_f32 v73, v74, v75\n\t"
        "v_mfma_f32_32x32x16_bf16 v[32:47], v[156:159], v[64:67], v[32:47]\n\t"
        "v_exp_f32 v77, v77\n\t"
        "v_exp_f32 v78, v78\n\t"
        "v_exp_f32 v79, v79\n\t"
        "v_cvt_pk_bf16_f32 v74, v76, v77\n\t"
        "v_cvt_pk_bf16_f32 v75, v78, v79\n\t"
        "v_exp_f32 v80, v80\n\t"
        "v_exp_f32 v81, v81\n\t"
        "v_mfma_f32_32x32x16_bf16 v[32:47], v[160:163], v[72:75], v[32:47]\n\t"
        "v_exp_f32 v82, v82\n\t"
        "v_exp_f32 v83, v83\n\t"
        "v_cvt_pk_bf16_f32 v80, v80, v81\n\t"
        "v_exp_f32 v84, v84\n\t"
        "s_waitcnt vmcnt(3)\n\t"
        "v_cvt_pk_bf16_f32 v81, v82, v83\n\t"
        "v_mfma_f32_32x32x8_bf16 v[64:79], v[146:147], v[112:113], 0\n\t"
        "v_exp_f32 v85, v85\n\t"
        "v_exp_f32 v86, v86\n\t"
        "v_exp_f32 v87, v87\n\t"
        "v_cvt_pk_bf16_f32 v82, v84, v85\n\t"
        "v_exp_f32 v88, v88\n\t"
        "v_cvt_pk_bf16_f32 v83, v86, v87\n\t"
        "v_mfma_f32_32x32x16_bf16 v[64:79], v[136:139], v[96:99], v[64:79]\n\t"
        "v_exp_f32 v89, v89\n\t"
        "v_exp_f32 v90, v90\n\t"
        "v_exp_f32 v91, v91\n\t"
        "v_cvt_pk_bf16_f32 v88, v88, v89\n\t"
        "v_exp_f32 v92, v92\n\t"
        "v_cvt_pk_bf16_f32 v89, v90, v91\n\t"
        "v_mfma_f32_32x32x16_bf16 v[48:63], v[156:159], v[80:83], v[48:63]\n\t"
        "v_exp_f32 v93, v93\n\t"
        "v_exp_f32 v94, v94\n\t"
        "v_exp_f32 v95, v95\n\t"
        "v_cvt_pk_bf16_f32 v90, v92, v93\n\t"
        "v_cvt_pk_bf16_f32 v91, v94, v95\n\t"
        "v_exp_f32 v64, v64\n\t"
        "v_exp_f32 v65, v65\n\t"
        "v_mfma_f32_32x32x16_bf16 v[48:63], v[160:163], v[88:91], v[48:63]\n\t"
        "v_exp_f32 v66, v66\n\t"
        "v_exp_f32 v67, v67\n\t"
        "v_cvt_pk_bf16_f32 v64, v64, v65\n\t"
        "v_exp_f32 v68, v68\n\t"
        "v_cvt_pk_bf16_f32 v65, v66, v67\n\t"
        "v_mfma_f32_32x32x8_bf16 v[80:95], v[146:147], v[114:115], 0\n\t"
        "v_exp_f32 v69, v69\n\t"
        "v_exp_f32 v70, v70\n\t"
        "v_exp_f32 v71, v71\n\t"
        "v_cvt_pk_bf16_f32 v66, v68, v69\n\t"
        "v_exp_f32 v72, v72\n\t"
        "v_cvt_pk_bf16_f32 v67, v70, v71\n\t"
        "v_mfma_f32_32x32x16_bf16 v[80:95], v[136:139], v[100:103], v[80:95]\n\t"
        "v_exp_f32 v73, v73\n\t"
        "v_exp_f32 v74, v74\n\t"
        "v_exp_f32 v75, v75\n\t"
        "v_cvt_pk_bf16_f32 v72, v72, v73\n\t"
        "v_exp_f32 v76, v76\n\t"
        "v_cvt_pk_bf16_f32 v73, v74, v75\n\t"
        "v_mfma_f32_32x32x16_bf16 v[0:15], v[164:167], v[64:67], v[0:15]\n\t"
        "v_exp_f32 v77, v77\n\t"
        "v_exp_f32 v78, v78\n\t"
        "v_exp_f32 v79, v79\n\t"
        "v_cvt_pk_bf16_f32 v74, v76, v77\n\t"
        "v_cvt_pk_bf16_f32 v75, v78, v79\n\t"
        "v_exp_f32 v80, v80\n\t"
        "v_exp_f32 v81, v81\n\t"
        "v_mfma_f32_32x32x16_bf16 v[0:15], v[168:171], v[72:75], v[0:15]\n\t"
        "v_exp_f32 v82, v82\n\t"
        "v_exp_f32 v83, v83\n\t"
        "v_cvt_pk_bf16_f32 v80, v80, v81\n\t"
        "v_exp_f32 v84, v84\n\t"
        "v_cvt_pk_bf16_f32 v81, v82, v83\n\t"
        "v_mfma_f32_32x32x8_bf16 v[64:79], v[146:147], v[116:117], 0\n\t"
        "v_exp_f32 v85, v85\n\t"
        "v_exp_f32 v86, v86\n\t"
        "v_exp_f32 v87, v87\n\t"
        "v_cvt_pk_bf16_f32 v82, v84, v85\n\t"
        "v_exp_f32 v88, v88\n\t"
        "v_cvt_pk_bf16_f32 v83, v86, v87\n\t"
        "v_mfma_f32_32x32x16_bf16 v[64:79], v[136:139], v[104:107], v[64:79]\n\t"
        "v_exp_f32 v89, v89\n\t"
        "v_exp_f32 v90, v90\n\t"
        "v_exp_f32 v91, v91\n\t"
        "v_cvt_pk_bf16_f32 v88, v88, v89\n\t"
        "v_exp_f32 v92, v92\n\t"
        "v_cvt_pk_bf16_f32 v89, v90, v91\n\t"
        "v_mfma_f32_32x32x16_bf16 v[16:31], v[164:167], v[80:83], v[16:31]\n\t"
        "v_exp_f32 v93, v93\n\t"
        "v_exp_f32 v94, v94\n\t"
        "v_exp_f32 v95, v95\n\t"
        "v_cvt_pk_bf16_f32 v90, v92, v93\n\t"
        "v_cvt_pk_bf16_f32 v91, v94, v95\n\t"
        "v_exp_f32 v64, v64\n\t"
        "v_exp_f32 v65, v65\n\t"
        "v_mfma_f32_32x32x16_bf16 v[16:31], v[168:171], v[88:91], v[16:31]\n\t"
        "v_exp_f32 v66, v66\n\t"
        "v_exp_f32 v67, v67\n\t"
        "v_cvt_pk_bf16_f32 v64, v64, v65\n\t"
        "v_exp_f32 v68, v68\n\t"
        "v_cvt_pk_bf16_f32 v65, v66, v67\n\t"
        "v_mfma_f32_32x32x8_bf16 v[80:95], v[146:147], v[118:119], 0\n\t"
        "v_exp_f32 v69, v69\n\t"
        "v_exp_f32 v70, v70\n\t"
        "v_exp_f32 v71, v71\n\t"
        "v_cvt_pk_bf16_f32 v66, v68, v69\n\t"
        "v_exp_f32 v72, v72\n\t"
        "v_cvt_pk_bf16_f32 v67, v70, v71\n\t"
        "v_mfma_f32_32x32x16_bf16 v[80:95], v[136:139], v[108:111], v[80:95]\n\t"
        "v_exp_f32 v73, v73\n\t"
        "v_exp_f32 v74, v74\n\t"
        "v_exp_f32 v75, v75\n\t"
        "v_cvt_pk_bf16_f32 v72, v72, v73\n\t"
        "v_exp_f32 v76, v76\n\t"
        "v_cvt_pk_bf16_f32 v73, v74, v75\n\t"
        "v_mfma_f32_32x32x16_bf16 v[32:47], v[164:167], v[64:67], v[32:47]\n\t"
        "v_exp_f32 v77, v77\n\t"
        "v_exp_f32 v78, v78\n\t"
        "v_exp_f32 v79, v79\n\t"
        "v_cvt_pk_bf16_f32 v74, v76, v77\n\t"
        "v_cvt_pk_bf16_f32 v75, v78, v79\n\t"
        "v_exp_f32 v80, v80\n\t"
        "v_exp_f32 v81, v81\n\t"
        "v_mfma_f32_32x32x16_bf16 v[32:47], v[168:171], v[72:75], v[32:47]\n\t"
        "v_exp_f32 v82, v82\n\t"
        "v_exp_f32 v83, v83\n\t"
        "v_cvt_pk_bf16_f32 v80, v80, v81\n\t"
        "v_exp_f32 v84, v84\n\t"
        "s_waitcnt vmcnt(0)\n\t"
        "v_cvt_pk_bf16_f32 v81, v82, v83\n\t"
        "v_mfma_f32_32x32x8_bf16 v[64:79], v[146:147], v[120:121], 0\n\t"
        "v_exp_f32 v85, v85\n\t"
        "v_exp_f32 v86, v86\n\t"
        "v_exp_f32 v87, v87\n\t"
        "v_cvt_pk_bf16_f32 v82, v84, v85\n\t"
        "v_exp_f32 v88, v88\n\t"
        "v_cvt_pk_bf16_f32 v83, v86, v87\n\t"
        "v_mfma_f32_32x32x16_bf16 v[64:79], v[140:143], v[96:99], v[64:79]\n\t"
        "v_exp_f32 v89, v89\n\t"
        "v_exp_f32 v90, v90\n\t"
        "v_exp_f32 v91, v91\n\t"
        "v_cvt_pk_bf16_f32 v88, v88, v89\n\t"
        "v_exp_f32 v92, v92\n\t"
        "v_cvt_pk_bf16_f32 v89, v90, v91\n\t"
        "v_mfma_f32_32x32x16_bf16 v[48:63], v[164:167], v[80:83], v[48:63]\n\t"
        "v_exp_f32 v93, v93\n\t"
        "v_exp_f32 v94, v94\n\t"
        "v_exp_f32 v95, v95\n\t"
        "v_cvt_pk_bf16_f32 v90, v92, v93\n\t"
        "v_cvt_pk_bf16_f32 v91, v94, v95\n\t"
        "v_exp_f32 v64, v64\n\t"
        "v_exp_f32 v65, v65\n\t"
        "v_mfma_f32_32x32x16_bf16 v[48:63], v[168:171], v[88:91], v[48:63]\n\t"
        "v_exp_f32 v66, v66\n\t"
        "v_exp_f32 v67, v67\n\t"
        "v_cvt_pk_bf16_f32 v64, v64, v65\n\t"
        "v_exp_f32 v68, v68\n\t"
        "v_cvt_pk_bf16_f32 v65, v66, v67\n\t"
        "v_mfma_f32_32x32x8_bf16 v[80:95], v[146:147], v[122:123], 0\n\t"
        "v_exp_f32 v69, v69\n\t"
        "v_exp_f32 v70, v70\n\t"
        "v_exp_f32 v71, v71\n\t"
        "v_cvt_pk_bf16_f32 v66, v68, v69\n\t"
        "v_exp_f32 v72, v72\n\t"
        "v_cvt_pk_bf16_f32 v67, v70, v71\n\t"
        "v_mfma_f32_32x32x16_bf16 v[80:95], v[140:143], v[100:103], v[80:95]\n\t"
        "v_exp_f32 v73, v73\n\t"
        "v_exp_f32 v74, v74\n\t"
        "v_exp_f32 v75, v75\n\t"
        "v_cvt_pk_bf16_f32 v72, v72, v73\n\t"
        "v_exp_f32 v76, v76\n\t"
        "v_cvt_pk_bf16_f32 v73, v74, v75\n\t"
        "v_mfma_f32_32x32x16_bf16 v[0:15], v[172:175], v[64:67], v[0:15]\n\t"
        "ds_read_b32 v180, %[ca]\n\t"
        "v_exp_f32 v77, v77\n\t"
        "v_exp_f32 v78, v78\n\t"
        "v_exp_f32 v79, v79\n\t"
        "v_cvt_pk_bf16_f32 v74, v76, v77\n\t"
        "v_cvt_pk_bf16_f32 v75, v78, v79\n\t"
        "v_exp_f32 v80, v80\n\t"
        "s_waitcnt lgkmcnt(0)\n\t"
        "v_cmp_gt_u32 vcc, 2, v180\n\t"
        "s_cbranch_vccnz 7f\n\t"
        "6:\n\t"
        "v_exp_f32 v81, v81\n\t"
        "v_mfma_f32_32x32x16_bf16 v[0:15], v[176:179], v[72:75], v[0:15]\n\t"
        "v_exp_f32 v82, v82\n\t"
        "v_exp_f32 v83, v83\n\t"
        "v_cvt_pk_bf16_f32 v80, v80, v81\n\t"
        "v_exp_f32 v84, v84\n\t"
        "v_cvt_pk_bf16_f32 v81, v82, v83\n\t"
        "v_mfma_f32_32x32x8_bf16 v[64:79], v[146:147], v[124:125], 0\n\t"
        "v_exp_f32 v85, v85\n\t"
        "v_exp_f32 v86, v86\n\t"
        "v_exp_f32 v87, v87\n\t"
        "v_cvt_pk_bf16_f32 v82, v84, v85\n\t"
        "v_exp_f32 v88, v88\n\t"
        "v_cvt_pk_bf16_f32 v83, v86, v87\n\t"
        "v_mfma_f32_32x32x16_bf16 v[64:79], v[140:143], v[104:107], v[64:79]\n\t"
        "v_exp_f32 v89, v89\n\t"
        "v_exp_f32 v90, v90\n\t"
        "v_exp_f32 v91, v91\n\t"
        "v_cvt_pk_bf16_f32 v88, v88, v89\n\t"
        "v_exp_f32 v92, v92\n\t"
        "v_cvt_pk_bf16_f32 v89, v90, v91\n\t"
        "v_mfma_f32_32x32x16_bf16 v[16:31], v[172:175], v[80:83], v[16:31]\n\t"
        "v_exp_f32 v93, v93\n\t"
        "v_exp_f32 v94, v94\n\t"
        "v_exp_f32 v95, v95\n\t"
        "v_cvt_pk_bf16_f32 v90, v92, v93\n\t"
        "v_cvt_pk_bf16_f32 v91, v94, v95\n\t"
        "v_exp_f32 v64, v64\n\t"
        "v_exp_f32 v65, v65\n\t"
        "v_mfma_f32_32x32x16_bf16 v[16:31], v[176:179], v[88:91], v[16:31]\n\t"
        "v_exp_f32 v66, v66\n\t"
        "v_exp_f32 v67, v67\n\t"
        "v_cvt_pk_bf16_f32 v64, v64, v65\n\t"
        "v_exp_f32 v68, v68\n\t"
        "v_cvt_pk_bf16_f32 v65, v66, v67\n\t"
        "v_mfma_f32_32x32x8_bf16 v[80:95], v[146:147], v[126:127], 0\n\t"
        "v_exp_f32 v69, v69\n\t"
        "v_exp_f32 v70, v70\n\t"
        "v_exp_f32 v71, v71\n\t"
        "v_cvt_pk_bf16_f32 v66, v68, v69\n\t"
        "v_exp_f32 v72, v72\n\t"
        "v_cvt_pk_bf16_f32 v67, v70, v71\n\t"
        "v_mfma_f32_32x32x16_bf16 v[80:95], v[140:143], v[108:111], v[80:95]\n\t"
        "ds_write_b128 %[pw], v[0:3] offset:0\n\t"
        "v_exp_f32 v73, v73\n\t"
        "v_exp_f32 v74, v74\n\t"
        "ds_write_b128 %[pw], v[4:7] offset:1024\n\t"
        "v_exp_f32 v75, v75\n\t"
        "v_cvt_pk_bf16_f32 v72, v72, v73\n\t"
        "v_exp_f32 v76, v76\n\t"
        "ds_write_b128 %[pw], v[8:11] offset:2048\n\t"
        "v_cvt_pk_bf16_f32 v73, v74, v75\n\t"
        "v_mfma_f32_32x32x16_bf16 v[32:47], v[172:175], v[64:67], v[32:47]\n\t"
        "v_exp_f32 v77, v77\n\t"
        "v_exp_f32 v78, v78\n\t"
        "v_exp_f32 v79, v79\n\t"
        "v_cvt_pk_bf16_f32 v74, v76, v77\n\t"
        "v_cvt_pk_bf16_f32 v75, v78, v79\n\t"
        "v_exp_f32 v80, v80\n\t"
        "ds_write_b128 %[pw], v[16:19] offset:3072\n\t"
        "v_exp_f32 v81, v81\n\t"
        "ds_write_b128 %[pw], v[20:23] offset:4096\n\t"
        "v_mfma_f32_32x32x16_bf16 v[32:47], v[176:179], v[72:75], v[32:47]\n\t"
        "v_exp_f32 v82, v82\n\t"
        "ds_write_b128 %[pw], v[24:27] offset:5120\n\t"
        "v_exp_f32 v83, v83\n\t"
        "v_cvt_pk_bf16_f32 v80, v80, v81\n\t"
        "v_exp_f32 v84, v84\n\t"
        "v_cvt_pk_bf16_f32 v81, v82, v83\n\t"
        "v_exp_f32 v85, v85\n\t"
        "v_exp_f32 v86, v86\n\t"
        "v_exp_f32 v87, v87\n\t"
        "v_cvt_pk_bf16_f32 v82, v84, v85\n\t"
        "v_exp_f32 v88, v88\n\t"
        "v_cvt_pk_bf16_f32 v83, v86, v87\n\t"
        "s_waitcnt lgkmcnt(0)\n\t"
        "s_mov_b64 exec, 1\n\t"
        "ds_add_u32 %[ca], %[one] offset:8\n\t"
        "s_mov_b64 exec, -1\n\t"
        "v_exp_f32 v89, v89\n\t"
        "v_exp_f32 v90, v90\n\t"
        "ds_read_b128 v[96:99], %[pr] offset:0\n\t"
        "ds_read_b128 v[100:103], %[pr] offset:1024\n\t"
        "v_exp_f32 v91, v91\n\t"
        "v_cvt_pk_bf16_f32 v88, v88, v89\n\t"
        "v_exp_f32 v92, v92\n\t"
        "ds_read_b128 v[104:107], %[pr] offset:2048\n\t"
        "ds_read_b128 v[108:111], %[pr] offset:3072\n\t"
        "v_cvt_pk_bf16_f32 v89, v90, v91\n\t"
        "v_mfma_f32_32x32x16_bf16 v[48:63], v[172:175], v[80:83], v[48:63]\n\t"
        "ds_read_b128 v[112:115], %[pr] offset:4096\n\t"
        "ds_read_b128 v[116:119], %[pr] offset:5120\n\t"
        "v_exp_f32 v93, v93\n\t"
        "v_exp_f32 v94, v94\n\t"
        "v_exp_f32 v95, v95\n\t"
        "v_cvt_pk_bf16_f32 v90, v92, v93\n\t"
        "v_cvt_pk_bf16_f32 v91, v94, v95\n\t"
        "s_cmp_lg_u32 0, 0\n\t"
        "s_nop 0\n\t"
        "v_mfma_f32_32x32x16_bf16 v[48:63], v[176:179], v[88:91], v[48:63]\n\t"
        "8:\n\t"
        "ds_read_b32 v180, %[ca] offset:8\n\t"
        "ds_read_b128 v[120:123], %[pr] offset:24576\n\t"
        "ds_read_b128 v[124:127], %[pr] offset:25600\n\t"
        "ds_read_b128 v[128:131], %[pr] offset:26624\n\t"
        "ds_read_b128 v[132:135], %[pr] offset:27648\n\t"
        "ds_read_b128 v[136:139], %[pr] offset:28672\n\t"
        "ds_read_b128 v[140:143], %[pr] offset:29696\n\t"
        "s_cbranch_scc1 9f\n\t"
        "s_waitcnt lgkmcnt(7)\n\t"
        "v_pk_add_f32 v[32:33], v[32:33], v[96:97]\n\t"
        "v_pk_add_f32 v[34:35], v[34:35], v[98:99]\n\t"
        "v_pk_add_f32 v[36:37], v[36:37], v[100:101]\n\t"
        "v_pk_add_f32 v[38:39], v[38:39], v[102:103]\n\t"
        "v_pk_add_f32 v[40:41], v[40:41], v[104:105]\n\t"
        "v_pk_add_f32 v[42:43], v[42:43], v[106:107]\n\t"
        "s_nop 3\n\t"
        "v_pk_add_f32 v[48:49], v[48:49], v[108:109]\n\t"
        "v_pk_add_f32 v[50:51], v[50:51], v[110:111]\n\t"
        "v_pk_add_f32 v[52:53], v[52:53], v[112:113]\n\t"
        "v_pk_add_f32 v[54:55], v[54:55], v[114:115]\n\t"
        "v_pk_add_f32 v[56:57], v[56:57], v[116:117]\n\t"
        "v_pk_add_f32 v[58:59], v[58:59], v[118:119]\n\t"
        "s_cmp_eq_u32 0, 0\n\t"
        "9:\n\t"
        "s_waitcnt lgkmcnt(0)\n\t"
        "v_cmp_gt_u32 vcc, 2, v180\n\t"
        "s_cbranch_vccz 10f\n\t"
        "s_sub_u32 %[cnt], %[cnt], 1\n\t"
        "s_cmp_lg_u32 %[cnt], 0xff000000\n\t"
        "s_cbranch_scc1 8b\n\t"
        "10:\n\t"
        "v_pk_add_f32 v[32:33], v[32:33], v[120:121]\n\t"
        "v_pk_add_f32 v[34:35], v[34:35], v[122:123]\n\t"
        "v_pk_add_f32 v[36:37], v[36:37], v[124:125]\n\t"
        "v_pk_add_f32 v[38:39], v[38:39], v[126:127]\n\t"
        "v_pk_add_f32 v[40:41], v[40:41], v[128:129]\n\t"
        "v_pk_add_f32 v[42:43], v[42:43], v[130:131]\n\t"
        "v_pk_add_f32 v[48:49], v[48:49], v[132:133]\n\t"
        "v_pk_add_f32 v[50:51], v[50:51], v[134:135]\n\t"
        "v_pk_add_f32 v[52:53], v[52:53], v[136:137]\n\t"
        "v_pk_add_f32 v[54:55], v[54:55], v[138:139]\n\t"
        "v_pk_add_f32 v[56:57], v[56:57], v[140:141]\n\t"
        "v_pk_add_f32 v[58:59], v[58:59], v[142:143]\n\t"
        "s_branch 11f\n\t"
        "7:\n\t"
        "s_sleep 2\n\t"
        "ds_read_b32 v180, %[ca]\n\t"
        "s_waitcnt lgkmcnt(0)\n\t"
        "v_cmp_gt_u32 vcc, 2, v180\n\t"
        "s_cbranch_vccz 6b\n\t"
        "s_sub_u32 %[cnt], %[cnt], 1\n\t"
        "s_cmp_lg_u32 %[cnt], 0xff000000\n\t"
        "s_cbranch_scc1 7b\n\t"
        "s_branch 6b\n\t"
        "11:\n\t"
        : "=&{v[32:47]}"(o2), "=&{v[48:63]}"(o3), [sk] "+s"(sk), [sv] "+s"(sv), [cnt] "+s"(cnt)
        : ATT_IN, [pw] "v"(pw), [pr] "v"(pr), [ca] "v"(ca), [one] "v"(1u)
        : "memory", "scc", "vcc", "v0", "v1", "v2", "v3", "v4", "v5", "v6", "v7", "v8", "v9", "v10", "v11", "v12", "v13", "v14", "v15", "v16", "v17", "v18", "v19", "v20", "v21", "v22", "v23", "v24", "v25", "v26", "v27", "v28", "v29", "v30", "v31", "v64", "v65", "v66", "v67", "v68", "v69", "v70", "v71", "v72", "v73", "v74", "v75", "v76", "v77", "v78", "v79", "v80", "v81", "v82", "v83", "v84", "v85", "v86", "v87", "v88", "v89", "v90", "v91", "v92", "v93", "v94", "v95", "v96", "v97", "v98", "v99", "v100", "v101", "v102", "v103", "v104", "v105", "v106", "v107", "v108", "v109", "v110", "v111", "v112", "v113", "v114", "v115", "v116", "v117", "v118", "v119", "v120", "v121", "v122", "v123", "v124", "v125", "v126", "v127", "v128", "v129", "v130", "v131", "v132", "v133", "v134", "v135", "v136", "v137", "v138", "v139", "v140", "v141", "v142", "v143", "v144", "v145", "v146", "v147", "v148", "v149", "v150", "v151", "v152", "v153", "v154", "v155", "v156", "v157", "v158", "v159", "v160", "v161", "v162", "v163", "v164", "v165", "v166", "v167", "v168", "v169", "v170", "v171", "v172", "v173", "v174", "v175", "v176", "v177", "v178", "v179", "v180");
}
DI void put3(LAS f32x4* p, const f32x16& o) { p[0] = (f32x4){o[0], o[1], o[2], o[3]}; p[64] = (f32x4){o[4], o[5], o[6], o[7]}; p[128] = (f32x4){o[8], o[9], o[10], o[11]}; }

DI void lds_arrive(LAS unsigned* p) { asm volatile("s_waitcnt lgkmcnt(0)\n\ts_mov_b64 exec, 1\n\tds_add_u32 %0, %1\n\ts_mov_b64 exec, -1" ::"v"(p), "v"(1u) : "memory"); }
DI void drain_and_flag(LAS unsigned* p, GAS u64* flag, u64 tag, bool on) {
    unsigned old;
    asm volatile("s_waitcnt vmcnt(0)\n\ts_mov_b64 exec, 1\n\tds_add_rtn_u32 %0, %1, %2\n\ts_mov_b64 exec, -1\n\ts_waitcnt lgkmcnt(0)" : "=&v"(old) : "v"(p), "v"(1u) : "memory");
    if (rfl(old) == 3u && on && (threadIdx.x & 63) == 0) __hip_atomic_store(flag, tag, RLX_AGENT);
}

__global__ __launch_bounds__(512, 2) void k_main(const char* xA, const char* wA, const u16* wphi, const char* woA, const char* xB, const char* wB, const char* woB, char* ws, char* out) {
    extern __shared__ __attribute__((aligned(16))) char lds[];
    Loader L; const char *xb, *wb; int ks;
    asm volatile(
        "s_load_dwordx4 s[40:43], %[kp], 0x20\n\t"
        "s_lshl_b32 s45, %[bid], 4\n\t"
        "s_and_b32 s45, s45, 0x380\n\t"
        "v_lshl_or_b32 %[x0], %[tid], 10, s45\n\t"
        "v_cmp_gt_u32 vcc, %[npf], %[tid]\n\t"
        "s_getpc_b64 s[56:57]\n\t"
        "s_mov_b32 s47, m0\n\t"
        "s_mov_b32 m0, %[junk]\n\t"
        "s_and_saveexec_b64 s[58:59], vcc\n\t"
        "s_cbranch_execz 1f\n\t"
        "global_load_lds_dword %[x0], s[56:57]\n\t"
        "1:\n\t"
        "s_mov_b64 exec, s[58:59]\n\t"
        "v_readfirstlane_b32 s48, %[tid]\n\t"
        "s_lshr_b32 s49, %[bid], 5\n\t"
        "s_bfe_u32 s50, %[bid], 0x20003\n\t"
        "s_lshl_b32 s51, %[bid], 3\n\t"
        "s_and_b32 s51, s51, 8\n\t"
        "s_add_u32 s51, s51, s49\n\t"
        "s_lshl_b32 s52, %[bid], 1\n\t"
        "s_and_b32 s52, s52, 12\n\t"
        "s_or_b32 s52, s52, s50\n\t"
        "s_lshl_b32 s45, s50, 2\n\t"
        "s_lshr_b32 s45, 0x8530, s45\n\t"
        "s_and_b32 s45, s45, 15\n\t"
        "s_add_u32 s44, s45, s49\n\t"
        "s_sub_u32 s45, s44, 10\n\t"
        "s_min_u32 s44, s44, s45\n\t"
        "s_mul_i32 s52, s52, 0x28000\n\t"
        "s_lshl_b32 s53, s51, 8\n\t"
        "v_bfe_u32 %[x1], %[tid], 3, 5\n\t"
        "v_bfe_u32 %[w1], %[tid], 4, 4\n\t"
        "v_mul_u32_u24 %[x1], 0x500, %[x1]\n\t"
        "v_xor_b32 %[x2], %[w1], %[tid]\n\t"
        "v_and_b32 %[x2], 7, %[x2]\n\t"
        "v_lshl_or_b32 %[x0], %[x2], 4, %[x1]\n\t"
        "v_lshrrev_b32 %[w2], 1, %[w1]\n\t"
        "v_and_b32 %[w2], 4, %[w2]\n\t"
        "v_and_or_b32 %[w2], %[w1], 3, %[w2]\n\t"
        "v_lshlrev_b32 %[w2], 1, %[w2]\n\t"
        "v_xor_b32 %[w2], %[w2], %[tid]\n\t"
        "v_and_b32 %[w2], 15, %[w2]\n\t"
        "v_mul_u32_u24 %[w1], 0xf00, %[w1]\n\t"
        "v_lshl_or_b32 %[w0], %[w2], 4, %[w1]\n\t"
        "v_add_u32 %[x1], 0xa000, %[x0]\n\t"
        "v_add_u32 %[x2], 0x14000, %[x0]\n\t"
        "v_add_u32 %[x3], 0x1e000, %[x0]\n\t"
        "v_add_u32 %[w1], 0xf000, %[w0]\n\t"
        "v_add_u32 %[w2], 0x1e000, %[w0]\n\t"
        "v_add_u32 %[w3], 0x2d000, %[w0]\n\t"
        "s_bfe_u32 s54, s48, 0x20006\n\t"
        "s_lshl_b32 s54, s54, 10\n\t"
        "s_waitcnt lgkmcnt(0)\n\t"
        "s_add_u32 s40, s40, s52\n\t"
        "s_addc_u32 s41, s41, 0\n\t"
        "s_add_u32 s42, s42, s53\n\t"
        "s_addc_u32 s43, s43, 0\n\t"
        "s_cmp_lt_u32 s48, 256\n\t"
        "s_cbranch_scc1 2f\n\t"
        "s_cmp_eq_u32 s51, 15\n\t"
        "s_cbranch_scc1 2f\n\t"
        "s_mov_b32 m0, s54\n\t"
        "s_lshl_b32 s45, s44, 7\n\t"
        "s_mul_i32 s46, s44, 0x3c000\n\t"
        "s_add_u32 s56, s40, s45\n\t"
        "s_addc_u32 s57, s41, 0\n\t"
        "s_add_u32 s58, s42, s46\n\t"
        "s_addc_u32 s59, s43, 0\n\t"
        "global_load_lds_dwordx4 %[x0], s[56:57]\n\t"
        "s_add_u32 m0, m0, 0x1000\n\t"
        "s_nop 0\n\t"
        "global_load_lds_dwordx4 %[x1], s[56:57]\n\t"
        "s_add_u32 m0, m0, 0x1000\n\t"
        "s_nop 0\n\t"
        "global_load_lds_dwordx4 %[x2], s[56:57]\n\t"
        "s_add_u32 m0, m0, 0x1000\n\t"
        "s_nop 0\n\t"
        "global_load_lds_dwordx4 %[x3], s[56:57]\n\t"
        "s_add_u32 m0, m0, 0x1000\n\t"
        "s_nop 0\n\t"
        "global_load_lds_dwordx4 %[w0], s[58:59]\n\t"
        "s_add_u32 m0, m0, 0x1000\n\t"
        "s_nop 0\n\t"
        "global_load_lds_dwordx4 %[w1], s[58:59]\n\t"
        "s_add_u32 m0, m0, 0x1000\n\t"
        "s_nop 0\n\t"
        "global_load_lds_dwordx4 %[w2], s[58:59]\n\t"
        "s_add_u32 m0, m0, 0x1000\n\t"
        "s_nop 0\n\t"
        "global_load_lds_dwordx4 %[w3], s[58:59]\n\t"
        "s_add_u32 s44, s44, 1\n\t"
        "s_cmp_eq_u32 s44, 10\n\t"
        "s_cselect_b32 s44, 0, s44\n\t"
        "s_or_b32 m0, s54, 0x8000\n\t"
        "s_lshl_b32 s45, s44, 7\n\t"
        "s_mul_i32 s46, s44, 0x3c000\n\t"
        "s_add_u32 s56, s40, s45\n\t"
        "s_addc_u32 s57, s41, 0\n\t"
        "s_add_u32 s58, s42, s46\n\t"
        "s_addc_u32 s59, s43, 0\n\t"
        "global_load_lds_dwordx4 %[x0], s[56:57]\n\t"
        "s_add_u32 m0, m0, 0x1000\n\t"
        "s_nop 0\n\t"
        "global_load_lds_dwordx4 %[x1], s[56:57]\n\t"
        "s_add_u32 m0, m0, 0x1000\n\t"
        "s_nop 0\n\t"
        "global_load_lds_dwordx4 %[x2], s[56:57]\n\t"
        "s_add_u32 m0, m0, 0x1000\n\t"
        "s_nop 0\n\t"
        "global_load_lds_dwordx4 %[x3], s[56:57]\n\t"
        "s_add_u32 m0, m0, 0x1000\n\t"
        "s_nop 0\n\t"
        "global_load_lds_dwordx4 %[w0], s[58:59]\n\t"
        "s_add_u32 m0, m0, 0x1000\n\t"
        "s_nop 0\n\t"
        "global_load_lds_dwordx4 %[w1], s[58:59]\n\t"
        "s_add_u32 m0, m0, 0x1000\n\t"
        "s_nop 0\n\t"
        "global_load_lds_dwordx4 %[w2], s[58:59]\n\t"
        "s_add_u32 m0, m0, 0x1000\n\t"
        "s_nop 0\n\t"
        "global_load_lds_dwordx4 %[w3], s[58:59]\n\t"
        "s_add_u32 s44, s44, 1\n\t"
        "s_cmp_eq_u32 s44, 10\n\t"
        "s_cselect_b32 s44, 0, s44\n\t"
        "2:\n\t"
        "s_mov_b32 m0, s47\n\t"
        : "=&{s[40:41]}"(xb), "=&{s[42:43]}"(wb), "=&{s44}"(ks), [x0] "=&v"(L.xo[0]), [x1] "=&v"(L.xo[1]), [x2] "=&v"(L.xo[2]), [x3] "=&v"(L.xo[3]), [w0] "=&v"(L.wo[0]), [w1] "=&v"(L.wo[1]), [w2] "=&v"(L.wo[2]), [w3] "=&v"(L.wo[3])
        : [kp] "s"(__builtin_amdgcn_kernarg_segment_ptr()), [bid] "s"(blockIdx.x), [tid] "v"(threadIdx.x), [npf] "i"(CODE_BYTES / 1024), [junk] "i"(L_JUNK)
        : "memory", "scc", "vcc", "s45", "s46", "s47", "s48", "s49", "s50", "s51", "s52", "s53", "s54", "s55", "s56", "s57", "s58", "s59");
    const int xcd = blockIdx.x & 7, j = blockIdx.x >> 3;
    const int tid = threadIdx.x, lane = tid & 63, wid = rfl(tid >> 6), r32 = lane & 31, h = lane >> 5;
    LAS unsigned* ctl = (LAS unsigned*)(lds + L_CTL);
    const int ml = j & 3, nl = j >> 2, mt = (xcd >> 1) * 4 + ml, nt = (xcd & 1) * 8 + nl;
    const bool has_tile = nt < 15;
    const int w4 = wid & 3;
    const int head = xcd * 4 + (j >> 3), rb = j & 7, odd = head & 1, hp = head >> 1;
    const unsigned c0 = (40 * hp + 16 * odd) >> 7, c1 = (40 * hp + 16 * odd + 23) >> 7, cv0 = (20 * head) >> 7, cv1 = (20 * head + 19) >> 7;
    const unsigned ctab = (5 + c0) | (5 + c1) << 4 | (10 + cv0) << 8 | (10 + cv1) << 12 | (c0 | c1 << 4) * 0x1010000u;
    const int rg = wid & 1, kh = (wid >> 1) & 1;
    if (!has_tile) {
        const unsigned o = (mt * 512 + tid) * 128; unsigned sv;
#pragma unroll 1
        for (int i = 0; i < 6; i++) {
            const char* p = i < 3 ? wA + i * 1048576 : xA + (i - 3) * 1048576;
            if (o < (i == 2 ? 2457600u - 2097152u : (i == 5 ? 2621440u - 2097152u : 1048576u)))
                asm volatile("s_mov_b32 %0, m0\n\ts_mov_b32 m0, %3\n\ts_nop 0\n\tglobal_load_lds_dword %1, %2\n\ts_mov_b32 m0, %0" : "=&s"(sv) : "v"(o), "s"(p), "i"(L_JUNK) : "memory");
        }
    }
    if (__builtin_expect(wid >= 4, 1)) {
        {
            const char* src = nullptr;
            if (wid == 4) { if (lane < 2) src = ws + WS_FLG + lane * 4; else if (lane < 32) ctl[lane] = 0u; }
            else if (wid == 6) { if (lane < 12) src = ws + lane * 2097152 + (lane == 11 ? 0 : j * 128); else if (lane < 15) src = out + (lane - 12) * 2097152; else if (lane < 17) src = lane == 15 ? woA : woB; }
            unsigned sv;
            if (wid == 5) { if (lane < 50) asm volatile("s_mov_b32 %0, m0\n\ts_mov_b32 m0, %3\n\ts_nop 0\n\tglobal_load_lds_dwordx4 %1, %2\n\ts_mov_b32 m0, %0" : "=&s"(sv) : "v"(lane * 16), "s"(wphi + head * 400), "s"(L_WPHI) : "memory"); }
            else if (src) asm volatile("s_mov_b32 %0, m0\n\ts_mov_b32 m0, %2\n\ts_nop 0\n\tglobal_load_lds_dword %1, off sc1\n\ts_mov_b32 m0, %0" : "=&s"(sv) : "v"(src), "s"(wid == 4 ? L_CTL : L_JUNK) : "memory");
            asm volatile("s_waitcnt lgkmcnt(0)" ::: "memory");
        }
        xb = (const char*)(((u64)rfl((u64)xb >> 32) << 32) | rfl((unsigned)(u64)xb)); wb = (const char*)(((u64)rfl((u64)wb >> 32) << 32) | rfl((unsigned)(u64)wb)); ks = rfl(ks);
        if (has_tile) { stage_next(L, xb + ks * 128, wb + ks * (64 * 3840), 65536 + w4 * 1024); ks = ks == 9 ? 0 : ks + 1; }
        const int vd = vdim(r32);
        AttLane A;
        { const unsigned cha = (5 * hp + 3 * odd + h) * 32768, chb = (5 * hp + 2) * 32768 + odd * 8;
          const unsigned tq = r32 * 16, tk = ((r32 & 7) | ((r32 & 8) << 1) | ((r32 & 16) >> 1)) * 16;
          A.qa = cha + tq; A.qb0 = h ? OOB : chb + tq; A.qb1 = h ? chb + tq : OOB; A.ka = cha + tk; A.kb = chb + tk + h * 512; A.vo = r32 <= 20 ? vofs(head, vd, h) : OOB; }
        asm volatile("" : "+v"(A.qa), "+v"(A.qb0), "+v"(A.qb1), "+v"(A.ka), "+v"(A.kb), "+v"(A.vo));
        unsigned so = (head * 2 + h) * 32768 + (rb * 256 + r32) * 16, sb = (lane & 33) == 0 ? O_B + head * 16384 + (rb * 256 + lane) * 8 : OOB;
        unsigned fidx = F_B + ((ctab >> ((lane >> 3) * 4)) & 15) * 16 + (lane < 32 ? (lane & 7) : 2 * rb + (lane & 1));
        const int obr = j >> 4, orow = xcd * 256 + ((j >> 3) & 1) * 128, ocol = (j & 7) * 80;
        unsigned pe = L_PE + rg * 12288 + lane * 16, it = w4 * 53 + lane, kd = L_KVW + it * 8 + (it / 5) * 8, kp = L_KVW + (vd * 2 + h) * 48, wv = (lane / 10) * 1280 + (lane % 10) * 16, w1, w2, w3, oa, ob, od;
        it = (lane < 53 && it < 210) ? it * 16 : OOB; so += w4 * 1024; sb += w4 * 512;
        { const int g = lane >> 4, q = (lane & 15) >> 2, p = lane & 3, tok = orow + w4 * 32 + (lane & 15);
          w1 = (20 * (g >> 1) + ((g & 1) ? 12 : 0) + q) * 160 + p * 8; w2 = (20 * (g >> 1) + ((g & 1) ? 8 : 4) + q) * 160 + p * 8; w3 = (20 * g + 16 + q) * 160 + p * 8;
          oa = g * 32768 + tok * 16; ob = O_B + g * 16384 + tok * 8; od = (orow + w4 * 32 + r32) * 2560 + (obr * 640 + ocol + 8 * h) * 2; }
        asm volatile("" : "+v"(fidx), "+v"(pe), "+v"(it), "+v"(kd), "+v"(kp), "+v"(wv), "+v"(w1), "+v"(w2), "+v"(w3), "+v"(oa), "+v"(ob), "+v"(od), "+v"(so), "+v"(sb));
        u64 tag, first = 0;
        if (has_tile) {
#pragma unroll 1
            for (int s = 3; s < 20; s++) {
                if (s == 10) { xb = xA + mt * (128 * 1280); wb = wA + nt * 256; ks = (nl + (ml * 5 + 1) / 2) % 10; }
                const char* xs = xb + ks * 128; const char* wsrc = wb + ks * (64 * 3840); const unsigned dst = ((s & 3) << 15) + w4 * 1024;
                ks = ks == 9 ? 0 : ks + 1;
                stage_next(L, xs, wsrc, dst);
                if (s == 12 + DRAIN && wid == 4) { const u64 t = call_tag(ctl); if (lane == 0) __hip_atomic_store(flagp(ws, F_B + nt * 16 + mt), t, RLX_AGENT); }
            }
            tag = call_tag(ctl);
            if (wid == 4) first = __hip_atomic_load(flagp(ws, fidx), RLX_AGENT);
            asm volatile("s_waitcnt vmcnt(0)\n\ts_barrier" : "+v"(first)::"memory");
        } else {
            asm volatile("s_waitcnt vmcnt(0)\n\ts_barrier" ::: "memory");
            tag = call_tag(ctl);
        }
        if (wid == 4) { if (!__all(first == tag)) wait_flags(ws, fidx, tag); __hip_atomic_store(ctl + C_BOK, 1u, RLX_WG); }
        if (wid != 4) wait_lds<0>(ctl + C_BOK, 1u);
        f32x16 o[4];
        __builtin_amdgcn_s_setprio(2);
        attend(ws, SEC + kh * 16 * 512, 2 * SEC + kh * 16 * V_T, (rb * 8 + rg * 4) * 512, (rb * 8 + rg * 4 + 2) * 512, A, o);
        LAS f32x4* pp4 = (LAS f32x4*)pe;
        wait_lds<4>(ctl + C_PHQ, 4u);
        if (kh) {
            wait_lds<4>(ctl + C_PE + rg, 1u);
#pragma unroll
            for (int u = 0; u < 4; u++)
#pragma unroll
                for (int k = 0; k < 3; k++) pp4[u * 192 + k * 64] += (f32x4){o[u][4 * k], o[u][4 * k + 1], o[u][4 * k + 2], o[u][4 * k + 3]};
        } else {
#pragma unroll
            for (int u = 0; u < 4; u++) put3(pp4 + u * 192, o[u]);
        }
        lds_arrive(ctl + C_PE + rg);
        {
            if (wid == 4) { wait_flags(ws, F_KV + head * 8 + (lane & 7), tag); __hip_atomic_store(ctl + C_KVOK, 1u, RLX_WG); }
            else wait_lds<4>(ctl + C_KVOK, 1u);
            const __amdgpu_buffer_rsrc_t rs = __builtin_amdgcn_make_buffer_rsrc(ws + WS_KVP + head * 32768, 0, 32768, 0x00020000);
            f32x4 s[8];
#pragma unroll
            for (int w = 0; w < 8; w++) s[w] = __builtin_bit_cast(f32x4, ld16(rs, it, w * 4096));
            const f32x4 t4 = ((s[0] + s[1]) + (s[2] + s[3])) + ((s[4] + s[5]) + (s[6] + s[7]));
            if (it != OOB) *(LAS u32x2*)kd = (u32x2){pk2(t4[0], t4[1]), pk2(t4[2], t4[3])};
            lds_arrive(ctl + C_KVF);
            wait_lds<2>(ctl + C_KVF, 4u);
            const u32x4 a0 = *(const LAS u32x4*)kp, a1 = *(const LAS u32x4*)(kp + 16); const u32x2 a2 = *(const LAS u32x2*)(kp + 32);
            const __amdgpu_buffer_rsrc_t ro = __builtin_amdgcn_make_buffer_rsrc(ws + WS_O, 0, SEC, 0x00020000);
#pragma unroll
            for (int t = 0; t < 2; t++) {
                const LAS char* pp = (const LAS char*)lds + L_PHQ + (w4 * 2 + t) * 2560;
                const u32x4 p0 = *(const LAS u32x4*)(pp + lane * 16), p1 = *(const LAS u32x4*)(pp + 1024 + lane * 16); const u32x2 p2 = *(const LAS u32x2*)(pp + 2048 + lane * 8);
                const f32x16 z16 = {};
                const f32x16 n = MFMA8(a2, p2, MFMA16(a1, p1, MFMA16(a0, p0, z16)));
                store_o(ro, so + t * 512, sb + t * 256, n, __builtin_amdgcn_rcpf(n[12] + 1e-6f));
            }
            drain_and_flag(ctl + C_OA, flagp(ws, F_O + rb * 32 + head), tag, true);
        }
        {
            wait_lds<4>(ctl + C_OA, 4u);
            if (lane < 60) {
                const char* wsrc = (obr ? woB : woA) + ocol * 2 + w4 * 7680;
#pragma unroll 1
                for (int i = w4; i < 107; i += 4, wsrc += 4 * 7680) { unsigned sv;
                    if (i < 106 || lane < 40) asm volatile("s_mov_b32 %0, m0\n\ts_mov_b32 m0, %3\n\ts_nop 0\n\tglobal_load_lds_dwordx4 %1, %2\n\ts_mov_b32 m0, %0" "\n\ts_waitcnt vmcnt(3)" : "=&s"(sv) : "v"(wv), "s"(wsrc), "s"(i * 960) : "memory"); }
            }
            const u32x4 srd = mksrd(ws + WS_O + obr * SEC, SEC);
            unsigned sof;
            asm volatile("s_waitcnt vmcnt(0)" ::: "memory");
            lds_arrive(ctl + C_W);
            wait_lds<4>(ctl + C_W, 4u);
            if (wid == 4 && !obr) wait_flags(ws, F_O + xcd * 32 + r32, tag);
            wait_lds<4>(ctl + C_PL + rg, 1u);
            if (wid == 4) {
                wait_lds<0>(ctl + C_OB, 4u);
                if (obr) wait_flags(ws, F_O + 256 + xcd * 32 + r32, tag);
                __hip_atomic_store(ctl + C_OOK, 1u, RLX_WG);
            } else wait_lds<1>(ctl + C_OOK, 1u);
            f32x32 r0; f32x4 r1, r2;
            asm volatile(
            "s_mov_b32 %[so], 0\n\t"
            "buffer_load_dwordx4 v[0:3], %[oa], %[srd], %[so] offen offset:0 sc1\n\t"
            "buffer_load_dwordx4 v[4:7], %[oa], %[srd], %[so] offen offset:256 sc1\n\t"
            "s_add_u32 %[so], %[so], 0x20000\n\t"
            "buffer_load_dwordx4 v[8:11], %[oa], %[srd], %[so] offen offset:0 sc1\n\t"
            "buffer_load_dwordx4 v[12:15], %[oa], %[srd], %[so] offen offset:256 sc1\n\t"
            "s_add_u32 %[so], %[so], 0x20000\n\t"
            "buffer_load_dwordx4 v[16:19], %[oa], %[srd], %[so] offen offset:0 sc1\n\t"
            "buffer_load_dwordx4 v[20:23], %[oa], %[srd], %[so] offen offset:256 sc1\n\t"
            "s_add_u32 %[so], %[so], 0x20000\n\t"
            "buffer_load_dwordx4 v[24:27], %[oa], %[srd], %[so] offen offset:0 sc1\n\t"
            "buffer_load_dwordx4 v[28:31], %[oa], %[srd], %[so] offen offset:256 sc1\n\t"
            "s_add_u32 %[so], %[so], 0x20000\n\t"
            "buffer_load_dwordx4 v[32:35], %[oa], %[srd], %[so] offen offset:0 sc1\n\t"
            "buffer_load_dwordx4 v[36:39], %[oa], %[srd], %[so] offen offset:256 sc1\n\t"
            "s_add_u32 %[so], %[so], 0x20000\n\t"
            "buffer_load_dwordx4 v[40:43], %[oa], %[srd], %[so] offen offset:0 sc1\n\t"
            "buffer_load_dwordx4 v[44:47], %[oa], %[srd], %[so] offen offset:256 sc1\n\t"
            "s_add_u32 %[so], %[so], 0x20000\n\t"
            "ds_read_b64_tr_b16 v[120:121], %[w1] offset:0\n\t"
            "ds_read_b64_tr_b16 v[122:123], %[w2] offset:0\n\t"
            "ds_read_b64_tr_b16 v[124:125], %[w1] offset:32\n\t"
            "ds_read_b64_tr_b16 v[126:127], %[w2] offset:32\n\t"
            "ds_read_b64_tr_b16 v[128:129], %[w1] offset:64\n\t"
            "ds_read_b64_tr_b16 v[130:131], %[w2] offset:64\n\t"
            "ds_read_b64_tr_b16 v[132:133], %[w1] offset:96\n\t"
            "ds_read_b64_tr_b16 v[134:135], %[w2] offset:96\n\t"
            "ds_read_b64_tr_b16 v[136:137], %[w1] offset:128\n\t"
            "ds_read_b64_tr_b16 v[138:139], %[w2] offset:128\n\t"
            "s_waitcnt vmcnt(10) lgkmcnt(0)\n\t"
            "v_mfma_f32_16x16x32_bf16 v[80:83], v[120:123], v[0:3], 0\n\t"
            "ds_read_b64_tr_b16 v[140:141], %[w1] offset:6400\n\t"
            "ds_read_b64_tr_b16 v[142:143], %[w2] offset:6400\n\t"
            "v_mfma_f32_16x16x32_bf16 v[100:103], v[120:123], v[4:7], 0\n\t"
            "ds_read_b64_tr_b16 v[144:145], %[w1] offset:6432\n\t"
            "ds_read_b64_tr_b16 v[146:147], %[w2] offset:6432\n\t"
            "v_mfma_f32_16x16x32_bf16 v[84:87], v[124:127], v[0:3], 0\n\t"
            "ds_read_b64_tr_b16 v[148:149], %[w1] offset:6464\n\t"
            "ds_read_b64_tr_b16 v[150:151], %[w2] offset:6464\n\t"
            "v_mfma_f32_16x16x32_bf16 v[104:107], v[124:127], v[4:7], 0\n\t"
            "ds_read_b64_tr_b16 v[152:153], %[w1] offset:6496\n\t"
            "ds_read_b64_tr_b16 v[154:155], %[w2] offset:6496\n\t"
            "v_mfma_f32_16x16x32_bf16 v[88:91], v[128:131], v[0:3], 0\n\t"
            "ds_read_b64_tr_b16 v[156:157], %[w1] offset:6528\n\t"
            "ds_read_b64_tr_b16 v[158:159], %[w2] offset:6528\n\t"
            "v_mfma_f32_16x16x32_bf16 v[108:111], v[128:131], v[4:7], 0\n\t"
            "buffer_load_dwordx4 v[48:51], %[oa], %[srd], %[so] offen offset:0 sc1\n\t"
            "buffer_load_dwordx4 v[52:55], %[oa], %[srd], %[so] offen offset:256 sc1\n\t"
            "v_mfma_f32_16x16x32_bf16 v[92:95], v[132:135], v[0:3], 0\n\t"
            "s_add_u32 %[so], %[so], 0x20000\n\t"
            "v_mfma_f32_16x16x32_bf16 v[112:115], v[132:135], v[4:7], 0\n\t"
            "v_mfma_f32_16x16x32_bf16 v[96:99], v[136:139], v[0:3], 0\n\t"
            "v_mfma_f32_16x16x32_bf16 v[116:119], v[136:139], v[4:7], 0\n\t"
            "s_waitcnt vmcnt(10) lgkmcnt(0)\n\t"
            "v_mfma_f32_16x16x32_bf16 v[80:83], v[140:143], v[8:11], v[80:83]\n\t"
            "ds_read_b64_tr_b16 v[120:121], %[w1] offset:12800\n\t"
            "ds_read_b64_tr_b16 v[122:123], %[w2] offset:12800\n\t"
            "v_mfma_f32_16x16x32_bf16 v[100:103], v[140:143], v[12:15], v[100:103]\n\t"
            "ds_read_b64_tr_b16 v[124:125], %[w1] offset:12832\n\t"
            "ds_read_b64_tr_b16 v[126:127], %[w2] offset:12832\n\t"
            "v_mfma_f32_16x16x32_bf16 v[84:87], v[144:147], v[8:11], v[84:87]\n\t"
            "ds_read_b64_tr_b16 v[128:129], %[w1] offset:12864\n\t"
            "ds_read_b64_tr_b16 v[130:131], %[w2] offset:12864\n\t"
            "v_mfma_f32_16x16x32_bf16 v[104:107], v[144:147], v[12:15], v[104:107]\n\t"
            "ds_read_b64_tr_b16 v[132:133], %[w1] offset:12896\n\t"
            "ds_read_b64_tr_b16 v[134:135], %[w2] offset:12896\n\t"
            "v_mfma_f32_16x16x32_bf16 v[88:91], v[148:151], v[8:11], v[88:91]\n\t"
            "ds_read_b64_tr_b16 v[136:137], %[w1] offset:12928\n\t"
            "ds_read_b64_tr_b16 v[138:139], %[w2] offset:12928\n\t"
            "v_mfma_f32_16x16x32_bf16 v[108:111], v[148:151], v[12:15], v[108:111]\n\t"
            "buffer_load_dwordx4 v[56:59], %[oa], %[srd], %[so] offen offset:0 sc1\n\t"
            "buffer_load_dwordx4 v[60:63], %[oa], %[srd], %[so] offen offset:256 sc1\n\t"
            "v_mfma_f32_16x16x32_bf16 v[92:95], v[152:155], v[8:11], v[92:95]\n\t"
            "s_add_u32 %[so], %[so], 0x20000\n\t"
            "v_mfma_f32_16x16x32_bf16 v[112:115], v[152:155], v[12:15], v[112:115]\n\t"
            "v_mfma_f32_16x16x32_bf16 v[96:99], v[156:159], v[8:11], v[96:99]\n\t"
            "v_mfma_f32_16x16x32_bf16 v[116:119], v[156:159], v[12:15], v[116:119]\n\t"
            "s_waitcnt vmcnt(10) lgkmcnt(0)\n\t"
            "v_mfma_f32_16x16x32_bf16 v[80:83], v[120:123], v[16:19], v[80:83]\n\t"
            "ds_read_b64_tr_b16 v[140:141], %[w1] offset:19200\n\t"
            "ds_read_b64_tr_b16 v[142:143], %[w2] offset:19200\n\t"
            "v_mfma_f32_16x16x32_bf16 v[100:103], v[120:123], v[20:23], v[100:103]\n\t"
            "ds_read_b64_tr_b16 v[144:145], %[w1] offset:19232\n\t"
            "ds_read_b64_tr_b16 v[146:147], %[w2] offset:19232\n\t"
            "v_mfma_f32_16x16x32_bf16 v[84:87], v[124:127], v[16:19], v[84:87]\n\t"
            "ds_read_b64_tr_b16 v[148:149], %[w1] offset:19264\n\t"
            "ds_read_b64_tr_b16 v[150:151], %[w2] offset:19264\n\t"
            "v_mfma_f32_16x16x32_bf16 v[104:107], v[124:127], v[20:23], v[104:107]\n\t"
            "ds_read_b64_tr_b16 v[152:153], %[w1] offset:19296\n\t"
            "ds_read_b64_tr_b16 v[154:155], %[w2] offset:19296\n\t"
            "v_mfma_f32_16x16x32_bf16 v[88:91], v[128:131], v[16:19], v[88:91]\n\t"
            "ds_read_b64_tr_b16 v[156:157], %[w1] offset:19328\n\t"
            "ds_read_b64_tr_b16 v[158:159], %[w2] offset:19328\n\t"
            "v_mfma_f32_16x16x32_bf16 v[108:111], v[128:131], v[20:23], v[108:111]\n\t"
            "buffer_load_dwordx4 v[64:67], %[oa], %[srd], %[so] offen offset:0 sc1\n\t"
            "buffer_load_dwordx4 v[68:71], %[oa], %[srd], %[so] offen offset:256 sc1\n\t"
            "v_mfma_f32_16x16x32_bf16 v[92:95], v[132:135], v[16:19], v[92:95]\n\t"
            "s_add_u32 %[so], %[so], 0x20000\n\t"
            "v_mfma_f32_16x16x32_bf16 v[112:115], v[132:135], v[20:23], v[112:115]\n\t"
            "v_mfma_f32_16x16x32_bf16 v[96:99], v[136:139], v[16:19], v[96:99]\n\t"
            "v_mfma_f32_16x16x32_bf16 v[116:119], v[136:139], v[20:23], v[116:119]\n\t"
            "s_waitcnt vmcnt(10) lgkmcnt(0)\n\t"
            "v_mfma_f32_16x16x32_bf16 v[80:83], v[140:143], v[24:27], v[80:83]\n\t"
            "ds_read_b64_tr_b16 v[120:121], %[w1] offset:25600\n\t"
            "ds_read_b64_tr_b16 v[122:123], %[w2] offset:25600\n\t"
            "v_mfma_f32_16x16x32_bf16 v[100:103], v[140:143], v[28:31], v[100:103]\n\t"
            "ds_read_b64_tr_b16 v[124:125], %[w1] offset:25632\n\t"
            "ds_read_b64_tr_b16 v[126:127], %[w2] offset:25632\n\t"
            "v_mfma_f32_16x16x32_bf16 v[84:87], v[144:147], v[24:27], v[84:87]\n\t"
            "ds_read_b64_tr_b16 v[128:129], %[w1] offset:25664\n\t"
            "ds_read_b64_tr_b16 v[130:131], %[w2] offset:25664\n\t"
            "v_mfma_f32_16x16x32_bf16 v[104:107], v[144:147], v[28:31], v[104:107]\n\t"
            "ds_read_b64_tr_b16 v[132:133], %[w1] offset:25696\n\t"
            "ds_read_b64_tr_b16 v[134:135], %[w2] offset:25696\n\t"
            "v_mfma_f32_16x16x32_bf16 v[88:91], v[148:151], v[24:27], v[88:91]\n\t"
            "ds_read_b64_tr_b16 v[136:137], %[w1] offset:25728\n\t"
            "ds_read_b64_tr_b16 v[138:139], %[w2] offset:25728\n\t"
            "v_mfma_f32_16x16x32_bf16 v[108:111], v[148:151], v[28:31], v[108:111]\n\t"
            "buffer_load_dwordx4 v[72:75], %[oa], %[srd], %[so] offen offset:0 sc1\n\t"
            "buffer_load_dwordx4 v[76:79], %[oa], %[srd], %[so] offen offset:256 sc1\n\t"
            "v_mfma_f32_16x16x32_bf16 v[92:95], v[152:155], v[24:27], v[92:95]\n\t"
            "s_add_u32 %[so], %[so], 0x20000\n\t"
            "v_mfma_f32_16x16x32_bf16 v[112:115], v[152:155], v[28:31], v[112:115]\n\t"
            "v_mfma_f32_16x16x32_bf16 v[96:99], v[156:159], v[24:27], v[96:99]\n\t"
            "v_mfma_f32_16x16x32_bf16 v[116:119], v[156:159], v[28:31], v[116:119]\n\t"
            "s_waitcnt vmcnt(10) lgkmcnt(0)\n\t"
            "v_mfma_f32_16x16x32_bf16 v[80:83], v[120:123], v[32:35], v[80:83]\n\t"
            "ds_read_b64_tr_b16 v[140:141], %[w1] offset:32000\n\t"
            "ds_read_b64_tr_b16 v[142:143], %[w2] offset:32000\n\t"
            "v_mfma_f32_16x16x32_bf16 v[100:103], v[120:123], v[36:39], v[100:103]\n\t"
            "ds_read_b64_tr_b16 v[144:145], %[w1] offset:32032\n\t"
            "ds_read_b64_tr_b16 v[146:147], %[w2] offset:32032\n\t"
            "v_mfma_f32_16x16x32_bf16 v[84:87], v[124:127], v[32:35], v[84:87]\n\t"
            "ds_read_b64_tr_b16 v[148:149], %[w1] offset:32064\n\t"
            "ds_read_b64_tr_b16 v[150:151], %[w2] offset:32064\n\t"
            "v_mfma_f32_16x16x32_bf16 v[104:107], v[124:127], v[36:39], v[104:107]\n\t"
            "ds_read_b64_tr_b16 v[152:153], %[w1] offset:32096\n\t"
            "ds_read_b64_tr_b16 v[154:155], %[w2] offset:32096\n\t"
            "v_mfma_f32_16x16x32_bf16 v[88:91], v[128:131], v[32:35], v[88:91]\n\t"
            "ds_read_b64_tr_b16 v[156:157], %[w1] offset:32128\n\t"
            "ds_read_b64_tr_b16 v[158:159], %[w2] offset:32128\n\t"
            "v_mfma_f32_16x16x32_bf16 v[108:111], v[128:131], v[36:39], v[108:111]\n\t"
            "buffer_load_dwordx4 v[0:3], %[oa], %[srd], %[so] offen offset:0 sc1\n\t"
            "buffer_load_dwordx4 v[4:7], %[oa], %[srd], %[so] offen offset:256 sc1\n\t"
            "v_mfma_f32_16x16x32_bf16 v[92:95], v[132:135], v[32:35], v[92:95]\n\t"
            "s_add_u32 %[so], %[so], 0x20000\n\t"
            "v_mfma_f32_16x16x32_bf16 v[112:115], v[132:135], v[36:39], v[112:115]\n\t"
            "v_mfma_f32_16x16x32_bf16 v[96:99], v[136:139], v[32:35], v[96:99]\n\t"
            "v_mfma_f32_16x16x32_bf16 v[116:119], v[136:139], v[36:39], v[116:119]\n\t"
            "s_waitcnt vmcnt(10) lgkmcnt(0)\n\t"
            "v_mfma_f32_16x16x32_bf16 v[80:83], v[140:143], v[40:43], v[80:83]\n\t"
            "ds_read_b64_tr_b16 v[120:121], %[w1] offset:38400\n\t"
            "ds_read_b64_tr_b16 v[122:123], %[w2] offset:38400\n\t"
            "v_mfma_f32_16x16x32_bf16 v[100:103], v[140:143], v[44:47], v[100:103]\n\t"
            "ds_read_b64_tr_b16 v[124:125], %[w1] offset:38432\n\t"
            "ds_read_b64_tr_b16 v[126:127], %[w2] offset:38432\n\t"
            "v_mfma_f32_16x16x32_bf16 v[84:87], v[144:147], v[40:43], v[84:87]\n\t"
            "ds_read_b64_tr_b16 v[128:129], %[w1] offset:38464\n\t"
            "ds_read_b64_tr_b16 v[130:131], %[w2] offset:38464\n\t"
            "v_mfma_f32_16x16x32_bf16 v[104:107], v[144:147], v[44:47], v[104:107]\n\t"
            "ds_read_b64_tr_b16 v[132:133], %[w1] offset:38496\n\t"
            "ds_read_b64_tr_b16 v[134:135], %[w2] offset:38496\n\t"
            "v_mfma_f32_16x16x32_bf16 v[88:91], v[148:151], v[40:43], v[88:91]\n\t"
            "ds_read_b64_tr_b16 v[136:137], %[w1] offset:38528\n\t"
            "ds_read_b64_tr_b16 v[138:139], %[w2] offset:38528\n\t"
            "v_mfma_f32_16x16x32_bf16 v[108:111], v[148:151], v[44:47], v[108:111]\n\t"
            "buffer_load_dwordx4 v[8:11], %[oa], %[srd], %[so] offen offset:0 sc1\n\t"
            "buffer_load_dwordx4 v[12:15], %[oa], %[srd], %[so] offen offset:256 sc1\n\t"
            "v_mfma_f32_16x16x32_bf16 v[92:95], v[152:155], v[40:43], v[92:95]\n\t"
            "s_add_u32 %[so], %[so], 0x20000\n\t"
            "v_mfma_f32_16x16x32_bf16 v[112:115], v[152:155], v[44:47], v[112:115]\n\t"
            "v_mfma_f32_16x16x32_bf16 v[96:99], v[156:159], v[40:43], v[96:99]\n\t"
            "v_mfma_f32_16x16x32_bf16 v[116:119], v[156:159], v[44:47], v[116:119]\n\t"
            "s_waitcnt vmcnt(10) lgkmcnt(0)\n\t"
            "v_mfma_f32_16x16x32_bf16 v[80:83], v[120:123], v[48:51], v[80:83]\n\t"
            "ds_read_b64_tr_b16 v[140:141], %[w1] offset:44800\n\t"
            "ds_read_b64_tr_b16 v[142:143], %[w2] offset:44800\n\t"
            "v_mfma_f32_16x16x32_bf16 v[100:103], v[120:123], v[52:55], v[100:103]\n\t"
            "ds_read_b64_tr_b16 v[144:145], %[w1] offset:44832\n\t"
            "ds_read_b64_tr_b16 v[146:147], %[w2] offset:44832\n\t"
            "v_mfma_f32_16x16x32_bf16 v[84:87], v[124:127], v[48:51], v[84:87]\n\t"
            "ds_read_b64_tr_b16 v[148:149], %[w1] offset:44864\n\t"
            "ds_read_b64_tr_b16 v[150:151], %[w2] offset:44864\n\t"
            "v_mfma_f32_16x16x32_bf16 v[104:107], v[124:127], v[52:55], v[104:107]\n\t"
            "ds_read_b64_tr_b16 v[152:153], %[w1] offset:44896\n\t"
            "ds_read_b64_tr_b16 v[154:155], %[w2] offset:44896\n\t"
            "v_mfma_f32_16x16x32_bf16 v[88:91], v[128:131], v[48:51], v[88:91]\n\t"
            "ds_read_b64_tr_b16 v[156:157], %[w1] offset:44928\n\t"
            "ds_read_b64_tr_b16 v[158:159], %[w2] offset:44928\n\t"
            "v_mfma_f32_16x16x32_bf16 v[108:111], v[128:131], v[52:55], v[108:111]\n\t"
            "buffer_load_dwordx4 v[16:19], %[oa], %[srd], %[so] offen offset:0 sc1\n\t"
            "buffer_load_dwordx4 v[20:23], %[oa], %[srd], %[so] offen offset:256 sc1\n\t"
            "v_mfma_f32_16x16x32_bf16 v[92:95], v[132:135], v[48:51], v[92:95]\n\t"
            "s_add_u32 %[so], %[so], 0x20000\n\t"
            "v_mfma_f32_16x16x32_bf16 v[112:115], v[132:135], v[52:55], v[112:115]\n\t"
            "v_mfma_f32_16x16x32_bf16 v[96:99], v[136:139], v[48:51], v[96:99]\n\t"
            "v_mfma_f32_16x16x32_bf16 v[116:119], v[136:139], v[52:55], v[116:119]\n\t"
            "s_waitcnt vmcnt(10) lgkmcnt(0)\n\t"
            "v_mfma_f32_16x16x32_bf16 v[80:83], v[140:143], v[56:59], v[80:83]\n\t"
            "ds_read_b64_tr_b16 v[120:121], %[w1] offset:51200\n\t"
            "ds_read_b64_tr_b16 v[122:123], %[w2] offset:51200\n\t"
            "v_mfma_f32_16x16x32_bf16 v[100:103], v[140:143], v[60:63], v[100:103]\n\t"
            "ds_read_b64_tr_b16 v[124:125], %[w1] offset:51232\n\t"
            "ds_read_b64_tr_b16 v[126:127], %[w2] offset:51232\n\t"
            "v_mfma_f32_16x16x32_bf16 v[84:87], v[144:147], v[56:59], v[84:87]\n\t"
            "ds_read_b64_tr_b16 v[128:129], %[w1] offset:51264\n\t"
            "ds_read_b64_tr_b16 v[130:131], %[w2] offset:51264\n\t"
            "v_mfma_f32_16x16x32_bf16 v[104:107], v[144:147], v[60:63], v[104:107]\n\t"
            "ds_read_b64_tr_b16 v[132:133], %[w1] offset:51296\n\t"
            "ds_read_b64_tr_b16 v[134:135], %[w2] offset:51296\n\t"
            "v_mfma_f32_16x16x32_bf16 v[88:91], v[148:151], v[56:59], v[88:91]\n\t"
            "ds_read_b64_tr_b16 v[136:137], %[w1] offset:51328\n\t"
            "ds_read_b64_tr_b16 v[138:139], %[w2] offset:51328\n\t"
            "v_mfma_f32_16x16x32_bf16 v[108:111], v[148:151], v[60:63], v[108:111]\n\t"
            "buffer_load_dwordx4 v[24:27], %[oa], %[srd], %[so] offen offset:0 sc1\n\t"
            "buffer_load_dwordx4 v[28:31], %[oa], %[srd], %[so] offen offset:256 sc1\n\t"
            "v_mfma_f32_16x16x32_bf16 v[92:95], v[152:155], v[56:59], v[92:95]\n\t"
            "s_add_u32 %[so], %[so], 0x20000\n\t"
            "v_mfma_f32_16x16x32_bf16 v[112:115], v[152:155], v[60:63], v[112:115]\n\t"
            "v_mfma_f32_16x16x32_bf16 v[96:99], v[156:159], v[56:59], v[96:99]\n\t"
            "v_mfma_f32_16x16x32_bf16 v[116:119], v[156:159], v[60:63], v[116:119]\n\t"
            "s_waitcnt vmcnt(10) lgkmcnt(0)\n\t"
            "v_mfma_f32_16x16x32_bf16 v[80:83], v[120:123], v[64:67], v[80:83]\n\t"
            "ds_read_b64_tr_b16 v[140:141], %[w1] offset:57600\n\t"
            "ds_read_b64_tr_b16 v[142:143], %[w2] offset:57600\n\t"
            "v_mfma_f32_16x16x32_bf16 v[100:103], v[120:123], v[68:71], v[100:103]\n\t"
            "ds_read_b64_tr_b16 v[144:145], %[w1] offset:57632\n\t"
            "ds_read_b64_tr_b16 v[146:147], %[w2] offset:57632\n\t"
            "v_mfma_f32_16x16x32_bf16 v[84:87], v[124:127], v[64:67], v[84:87]\n\t"
            "ds_read_b64_tr_b16 v[148:149], %[w1] offset:57664\n\t"
            "ds_read_b64_tr_b16 v[150:151], %[w2] offset:57664\n\t"
            "v_mfma_f32_16x16x32_bf16 v[104:107], v[124:127], v[68:71], v[104:107]\n\t"
            "ds_read_b64_tr_b16 v[152:153], %[w1] offset:57696\n\t"
            "ds_read_b64_tr_b16 v[154:155], %[w2] offset:57696\n\t"
            "v_mfma_f32_16x16x32_bf16 v[88:91], v[128:131], v[64:67], v[88:91]\n\t"
            "ds_read_b64_tr_b16 v[156:157], %[w1] offset:57728\n\t"
            "ds_read_b64_tr_b16 v[158:159], %[w2] offset:57728\n\t"
            "v_mfma_f32_16x16x32_bf16 v[108:111], v[128:131], v[68:71], v[108:111]\n\t"
            "buffer_load_dwordx4 v[32:35], %[oa], %[srd], %[so] offen offset:0 sc1\n\t"
            "buffer_load_dwordx4 v[36:39], %[oa], %[srd], %[so] offen offset:256 sc1\n\t"
            "v_mfma_f32_16x16x32_bf16 v[92:95], v[132:135], v[64:67], v[92:95]\n\t"
            "s_add_u32 %[so], %[so], 0x20000\n\t"
            "v_mfma_f32_16x16x32_bf16 v[112:115], v[132:135], v[68:71], v[112:115]\n\t"
            "v_mfma_f32_16x16x32_bf16 v[96:99], v[136:139], v[64:67], v[96:99]\n\t"
            "v_mfma_f32_16x16x32_bf16 v[116:119], v[136:139], v[68:71], v[116:119]\n\t"
            "v_add_u32 %[w1], 64000, %[w1]\n\t"
            "v_add_u32 %[w2], 64000, %[w2]\n\t"
            "s_waitcnt vmcnt(10) lgkmcnt(0)\n\t"
            "v_mfma_f32_16x16x32_bf16 v[80:83], v[140:143], v[72:75], v[80:83]\n\t"
            "ds_read_b64_tr_b16 v[120:121], %[w1] offset:0\n\t"
            "ds_read_b64_tr_b16 v[122:123], %[w2] offset:0\n\t"
            "v_mfma_f32_16x16x32_bf16 v[100:103], v[140:143], v[76:79], v[100:103]\n\t"
            "ds_read_b64_tr_b16 v[124:125], %[w1] offset:32\n\t"
            "ds_read_b64_tr_b16 v[126:127], %[w2] offset:32\n\t"
            "v_mfma_f32_16x16x32_bf16 v[84:87], v[144:147], v[72:75], v[84:87]\n\t"
            "ds_read_b64_tr_b16 v[128:129], %[w1] offset:64\n\t"
            "ds_read_b64_tr_b16 v[130:131], %[w2] offset:64\n\t"
            "v_mfma_f32_16x16x32_bf16 v[104:107], v[144:147], v[76:79], v[104:107]\n\t"
            "ds_read_b64_tr_b16 v[132:133], %[w1] offset:96\n\t"
            "ds_read_b64_tr_b16 v[134:135], %[w2] offset:96\n\t"
            "v_mfma_f32_16x16x32_bf16 v[88:91], v[148:151], v[72:75], v[88:91]\n\t"
            "ds_read_b64_tr_b16 v[136:137], %[w1] offset:128\n\t"
            "ds_read_b64_tr_b16 v[138:139], %[w2] offset:128\n\t"
            "v_mfma_f32_16x16x32_bf16 v[108:111], v[148:151], v[76:79], v[108:111]\n\t"
            "buffer_load_dwordx4 v[40:43], %[oa], %[srd], %[so] offen offset:0 sc1\n\t"
            "buffer_load_dwordx4 v[44:47], %[oa], %[srd], %[so] offen offset:256 sc1\n\t"
            "v_mfma_f32_16x16x32_bf16 v[92:95], v[152:155], v[72:75], v[92:95]\n\t"
            "s_mov_b32 %[so], 0\n\t"
            "v_mfma_f32_16x16x32_bf16 v[112:115], v[152:155], v[76:79], v[112:115]\n\t"
            "v_mfma_f32_16x16x32_bf16 v[96:99], v[156:159], v[72:75], v[96:99]\n\t"
            "v_mfma_f32_16x16x32_bf16 v[116:119], v[156:159], v[76:79], v[116:119]\n\t"
            "s_waitcnt vmcnt(10) lgkmcnt(0)\n\t"
            "v_mfma_f32_16x16x32_bf16 v[80:83], v[120:123], v[0:3], v[80:83]\n\t"
            "ds_read_b64_tr_b16 v[140:141], %[w1] offset:6400\n\t"
            "ds_read_b64_tr_b16 v[142:143], %[w2] offset:6400\n\t"
            "v_mfma_f32_16x16x32_bf16 v[100:103], v[120:123], v[4:7], v[100:103]\n\t"
            "ds_read_b64_tr_b16 v[144:145], %[w1] offset:6432\n\t"
            "ds_read_b64_tr_b16 v[146:147], %[w2] offset:6432\n\t"
            "v_mfma_f32_16x16x32_bf16 v[84:87], v[124:127], v[0:3], v[84:87]\n\t"
            "ds_read_b64_tr_b16 v[148:149], %[w1] offset:6464\n\t"
            "ds_read_b64_tr_b16 v[150:151], %[w2] offset:6464\n\t"
            "v_mfma_f32_16x16x32_bf16 v[104:107], v[124:127], v[4:7], v[104:107]\n\t"
            "ds_read_b64_tr_b16 v[152:153], %[w1] offset:6496\n\t"
            "ds_read_b64_tr_b16 v[154:155], %[w2] offset:6496\n\t"
            "v_mfma_f32_16x16x32_bf16 v[88:91], v[128:131], v[0:3], v[88:91]\n\t"
            "ds_read_b64_tr_b16 v[156:157], %[w1] offset:6528\n\t"
            "ds_read_b64_tr_b16 v[158:159], %[w2] offset:6528\n\t"
            "v_mfma_f32_16x16x32_bf16 v[108:111], v[128:131], v[4:7], v[108:111]\n\t"
            "buffer_load_dwordx2 v[48:49], %[ob], %[srd], %[so] offen offset:0 sc1\n\t"
            "buffer_load_dwordx2 v[52:53], %[ob], %[srd], %[so] offen offset:128 sc1\n\t"
            "v_mfma_f32_16x16x32_bf16 v[92:95], v[132:135], v[0:3], v[92:95]\n\t"
            "s_add_u32 %[so], %[so], 0x10000\n\t"
            "buffer_load_dwordx2 v[50:51], %[ob], %[srd], %[so] offen offset:0 sc1\n\t"
            "v_mfma_f32_16x16x32_bf16 v[112:115], v[132:135], v[4:7], v[112:115]\n\t"
            "buffer_load_dwordx2 v[54:55], %[ob], %[srd], %[so] offen offset:128 sc1\n\t"
            "s_add_u32 %[so], %[so], 0x10000\n\t"
            "v_mfma_f32_16x16x32_bf16 v[96:99], v[136:139], v[0:3], v[96:99]\n\t"
            "v_mfma_f32_16x16x32_bf16 v[116:119], v[136:139], v[4:7], v[116:119]\n\t"
            "s_waitcnt vmcnt(12) lgkmcnt(0)\n\t"
            "v_mfma_f32_16x16x32_bf16 v[80:83], v[140:143], v[8:11], v[80:83]\n\t"
            "ds_read_b64_tr_b16 v[120:121], %[w1] offset:12800\n\t"
            "ds_read_b64_tr_b16 v[122:123], %[w2] offset:12800\n\t"
            "v_mfma_f32_16x16x32_bf16 v[100:103], v[140:143], v[12:15], v[100:103]\n\t"
            "ds_read_b64_tr_b16 v[124:125], %[w1] offset:12832\n\t"
            "ds_read_b64_tr_b16 v[126:127], %[w2] offset:12832\n\t"
            "v_mfma_f32_16x16x32_bf16 v[84:87], v[144:147], v[8:11], v[84:87]\n\t"
            "ds_read_b64_tr_b16 v[128:129], %[w1] offset:12864\n\t"
            "ds_read_b64_tr_b16 v[130:131], %[w2] offset:12864\n\t"
            "v_mfma_f32_16x16x32_bf16 v[104:107], v[144:147], v[12:15], v[104:107]\n\t"
            "ds_read_b64_tr_b16 v[132:133], %[w1] offset:12896\n\t"
            "ds_read_b64_tr_b16 v[134:135], %[w2] offset:12896\n\t"
            "v_mfma_f32_16x16x32_bf16 v[88:91], v[148:151], v[8:11], v[88:91]\n\t"
            "ds_read_b64_tr_b16 v[136:137], %[w1] offset:12928\n\t"
            "ds_read_b64_tr_b16 v[138:139], %[w2] offset:12928\n\t"
            "v_mfma_f32_16x16x32_bf16 v[108:111], v[148:151], v[12:15], v[108:111]\n\t"
            "buffer_load_dwordx2 v[56:57], %[ob], %[srd], %[so] offen offset:0 sc1\n\t"
            "buffer_load_dwordx2 v[60:61], %[ob], %[srd], %[so] offen offset:128 sc1\n\t"
            "v_mfma_f32_16x16x32_bf16 v[92:95], v[152:155], v[8:11], v[92:95]\n\t"
            "s_add_u32 %[so], %[so], 0x10000\n\t"
            "buffer_load_dwordx2 v[58:59], %[ob], %[srd], %[so] offen offset:0 sc1\n\t"
            "v_mfma_f32_16x16x32_bf16 v[112:115], v[152:155], v[12:15], v[112:115]\n\t"
            "buffer_load_dwordx2 v[62:63], %[ob], %[srd], %[so] offen offset:128 sc1\n\t"
            "s_add_u32 %[so], %[so], 0x10000\n\t"
            "v_mfma_f32_16x16x32_bf16 v[96:99], v[156:159], v[8:11], v[96:99]\n\t"
            "v_mfma_f32_16x16x32_bf16 v[116:119], v[156:159], v[12:15], v[116:119]\n\t"
            "s_waitcnt vmcnt(14) lgkmcnt(0)\n\t"
            "v_mfma_f32_16x16x32_bf16 v[80:83], v[120:123], v[16:19], v[80:83]\n\t"
            "ds_read_b64_tr_b16 v[140:141], %[w1] offset:19200\n\t"
            "ds_read_b64_tr_b16 v[142:143], %[w2] offset:19200\n\t"
            "v_mfma_f32_16x16x32_bf16 v[100:103], v[120:123], v[20:23], v[100:103]\n\t"
            "ds_read_b64_tr_b16 v[144:145], %[w1] offset:19232\n\t"
            "ds_read_b64_tr_b16 v[146:147], %[w2] offset:19232\n\t"
            "v_mfma_f32_16x16x32_bf16 v[84:87], v[124:127], v[16:19], v[84:87]\n\t"
            "ds_read_b64_tr_b16 v[148:149], %[w1] offset:19264\n\t"
            "ds_read_b64_tr_b16 v[150:151], %[w2] offset:19264\n\t"
            "v_mfma_f32_16x16x32_bf16 v[104:107], v[124:127], v[20:23], v[104:107]\n\t"
            "ds_read_b64_tr_b16 v[152:153], %[w1] offset:19296\n\t"
            "ds_read_b64_tr_b16 v[154:155], %[w2] offset:19296\n\t"
            "v_mfma_f32_16x16x32_bf16 v[88:91], v[128:131], v[16:19], v[88:91]\n\t"
            "ds_read_b64_tr_b16 v[156:157], %[w1] offset:19328\n\t"
            "ds_read_b64_tr_b16 v[158:159], %[w2] offset:19328\n\t"
            "v_mfma_f32_16x16x32_bf16 v[108:111], v[128:131], v[20:23], v[108:111]\n\t"
            "buffer_load_dwordx2 v[64:65], %[ob], %[srd], %[so] offen offset:0 sc1\n\t"
            "buffer_load_dwordx2 v[68:69], %[ob], %[srd], %[so] offen offset:128 sc1\n\t"
            "v_mfma_f32_16x16x32_bf16 v[92:95], v[132:135], v[16:19], v[92:95]\n\t"
            "s_add_u32 %[so], %[so], 0x10000\n\t"
            "buffer_load_dwordx2 v[66:67], %[ob], %[srd], %[so] offen offset:0 sc1\n\t"
            "v_mfma_f32_16x16x32_bf16 v[112:115], v[132:135], v[20:23], v[112:115]\n\t"
            "buffer_load_dwordx2 v[70:71], %[ob], %[srd], %[so] offen offset:128 sc1\n\t"
            "s_add_u32 %[so], %[so], 0x10000\n\t"
            "v_mfma_f32_16x16x32_bf16 v[96:99], v[136:139], v[16:19], v[96:99]\n\t"
            "v_mfma_f32_16x16x32_bf16 v[116:119], v[136:139], v[20:23], v[116:119]\n\t"
            "s_waitcnt vmcnt(16) lgkmcnt(0)\n\t"
            "v_mfma_f32_16x16x32_bf16 v[80:83], v[140:143], v[24:27], v[80:83]\n\t"
            "ds_read_b64_tr_b16 v[120:121], %[w1] offset:25600\n\t"
            "ds_read_b64_tr_b16 v[122:123], %[w2] offset:25600\n\t"
            "v_mfma_f32_16x16x32_bf16 v[100:103], v[140:143], v[28:31], v[100:103]\n\t"
            "ds_read_b64_tr_b16 v[124:125], %[w1] offset:25632\n\t"
            "ds_read_b64_tr_b16 v[126:127], %[w2] offset:25632\n\t"
            "v_mfma_f32_16x16x32_bf16 v[84:87], v[144:147], v[24:27], v[84:87]\n\t"
            "ds_read_b64_tr_b16 v[128:129], %[w1] offset:25664\n\t"
            "ds_read_b64_tr_b16 v[130:131], %[w2] offset:25664\n\t"
            "v_mfma_f32_16x16x32_bf16 v[104:107], v[144:147], v[28:31], v[104:107]\n\t"
            "ds_read_b64_tr_b16 v[132:133], %[w1] offset:25696\n\t"
            "ds_read_b64_tr_b16 v[134:135], %[w2] offset:25696\n\t"
            "v_mfma_f32_16x16x32_bf16 v[88:91], v[148:151], v[24:27], v[88:91]\n\t"
            "ds_read_b64_tr_b16 v[136:137], %[w1] offset:25728\n\t"
            "ds_read_b64_tr_b16 v[138:139], %[w2] offset:25728\n\t"
            "v_mfma_f32_16x16x32_bf16 v[108:111], v[148:151], v[28:31], v[108:111]\n\t"
            "buffer_load_dwordx2 v[72:73], %[ob], %[srd], %[so] offen offset:0 sc1\n\t"
            "buffer_load_dwordx2 v[76:77], %[ob], %[srd], %[so] offen offset:128 sc1\n\t"
            "v_mfma_f32_16x16x32_bf16 v[92:95], v[152:155], v[24:27], v[92:95]\n\t"
            "s_add_u32 %[so], %[so], 0x10000\n\t"
            "buffer_load_dwordx2 v[74:75], %[ob], %[srd], %[so] offen offset:0 sc1\n\t"
            "v_mfma_f32_16x16x32_bf16 v[112:115], v[152:155], v[28:31], v[112:115]\n\t"
            "buffer_load_dwordx2 v[78:79], %[ob], %[srd], %[so] offen offset:128 sc1\n\t"
            "s_add_u32 %[so], %[so], 0x10000\n\t"
            "v_mfma_f32_16x16x32_bf16 v[96:99], v[156:159], v[24:27], v[96:99]\n\t"
            "v_mfma_f32_16x16x32_bf16 v[116:119], v[156:159], v[28:31], v[116:119]\n\t"
            "s_waitcnt vmcnt(18) lgkmcnt(0)\n\t"
            "v_mfma_f32_16x16x32_bf16 v[80:83], v[120:123], v[32:35], v[80:83]\n\t"
            "ds_read_b64_tr_b16 v[140:141], %[w1] offset:32000\n\t"
            "ds_read_b64_tr_b16 v[142:143], %[w2] offset:32000\n\t"
            "v_mfma_f32_16x16x32_bf16 v[100:103], v[120:123], v[36:39], v[100:103]\n\t"
            "ds_read_b64_tr_b16 v[144:145], %[w1] offset:32032\n\t"
            "ds_read_b64_tr_b16 v[146:147], %[w2] offset:32032\n\t"
            "v_mfma_f32_16x16x32_bf16 v[84:87], v[124:127], v[32:35], v[84:87]\n\t"
            "ds_read_b64_tr_b16 v[148:149], %[w1] offset:32064\n\t"
            "ds_read_b64_tr_b16 v[150:151], %[w2] offset:32064\n\t"
            "v_mfma_f32_16x16x32_bf16 v[104:107], v[124:127], v[36:39], v[104:107]\n\t"
            "ds_read_b64_tr_b16 v[152:153], %[w1] offset:32096\n\t"
            "ds_read_b64_tr_b16 v[154:155], %[w2] offset:32096\n\t"
            "v_mfma_f32_16x16x32_bf16 v[88:91], v[128:131], v[32:35], v[88:91]\n\t"
            "ds_read_b64_tr_b16 v[156:157], %[w1] offset:32128\n\t"
            "ds_read_b64_tr_b16 v[158:159], %[w2] offset:32128\n\t"
            "v_mfma_f32_16x16x32_bf16 v[108:111], v[128:131], v[36:39], v[108:111]\n\t"
            "v_mfma_f32_16x16x32_bf16 v[92:95], v[132:135], v[32:35], v[92:95]\n\t"
            "v_mfma_f32_16x16x32_bf16 v[112:115], v[132:135], v[36:39], v[112:115]\n\t"
            "v_mfma_f32_16x16x32_bf16 v[96:99], v[136:139], v[32:35], v[96:99]\n\t"
            "v_mfma_f32_16x16x32_bf16 v[116:119], v[136:139], v[36:39], v[116:119]\n\t"
            "s_waitcnt vmcnt(16) lgkmcnt(0)\n\t"
            "v_mfma_f32_16x16x32_bf16 v[80:83], v[140:143], v[40:43], v[80:83]\n\t"
            "ds_read_b64_tr_b16 v[120:121], %[w3] offset:0\n\t"
            "ds_read_b64_tr_b16 v[122:123], %[w3] offset:12800\n\t"
            "v_mfma_f32_16x16x32_bf16 v[100:103], v[140:143], v[44:47], v[100:103]\n\t"
            "ds_read_b64_tr_b16 v[124:125], %[w3] offset:32\n\t"
            "ds_read_b64_tr_b16 v[126:127], %[w3] offset:12832\n\t"
            "v_mfma_f32_16x16x32_bf16 v[84:87], v[144:147], v[40:43], v[84:87]\n\t"
            "ds_read_b64_tr_b16 v[128:129], %[w3] offset:64\n\t"
            "ds_read_b64_tr_b16 v[130:131], %[w3] offset:12864\n\t"
            "v_mfma_f32_16x16x32_bf16 v[104:107], v[144:147], v[44:47], v[104:107]\n\t"
            "ds_read_b64_tr_b16 v[132:133], %[w3] offset:96\n\t"
            "ds_read_b64_tr_b16 v[134:135], %[w3] offset:12896\n\t"
            "v_mfma_f32_16x16x32_bf16 v[88:91], v[148:151], v[40:43], v[88:91]\n\t"
            "ds_read_b64_tr_b16 v[136:137], %[w3] offset:128\n\t"
            "ds_read_b64_tr_b16 v[138:139], %[w3] offset:12928\n\t"
            "v_mfma_f32_16x16x32_bf16 v[108:111], v[148:151], v[44:47], v[108:111]\n\t"
            "v_mfma_f32_16x16x32_bf16 v[92:95], v[152:155], v[40:43], v[92:95]\n\t"
            "v_mfma_f32_16x16x32_bf16 v[112:115], v[152:155], v[44:47], v[112:115]\n\t"
            "v_mfma_f32_16x16x32_bf16 v[96:99], v[156:159], v[40:43], v[96:99]\n\t"
            "v_mfma_f32_16x16x32_bf16 v[116:119], v[156:159], v[44:47], v[116:119]\n\t"
            "s_waitcnt vmcnt(12) lgkmcnt(0)\n\t"
            "v_mfma_f32_16x16x32_bf16 v[80:83], v[120:123], v[48:51], v[80:83]\n\t"
            "ds_read_b64_tr_b16 v[140:141], %[w3] offset:25600\n\t"
            "ds_read_b64_tr_b16 v[142:143], %[w3] offset:38400\n\t"
            "v_mfma_f32_16x16x32_bf16 v[100:103], v[120:123], v[52:55], v[100:103]\n\t"
            "ds_read_b64_tr_b16 v[144:145], %[w3] offset:25632\n\t"
            "ds_read_b64_tr_b16 v[146:147], %[w3] offset:38432\n\t"
            "v_mfma_f32_16x16x32_bf16 v[84:87], v[124:127], v[48:51], v[84:87]\n\t"
            "ds_read_b64_tr_b16 v[148:149], %[w3] offset:25664\n\t"
            "ds_read_b64_tr_b16 v[150:151], %[w3] offset:38464\n\t"
            "v_mfma_f32_16x16x32_bf16 v[104:107], v[124:127], v[52:55], v[104:107]\n\t"
            "ds_read_b64_tr_b16 v[152:153], %[w3] offset:25696\n\t"
            "ds_read_b64_tr_b16 v[154:155], %[w3] offset:38496\n\t"
            "v_mfma_f32_16x16x32_bf16 v[88:91], v[128:131], v[48:51], v[88:91]\n\t"
            "ds_read_b64_tr_b16 v[156:157], %[w3] offset:25728\n\t"
            "ds_read_b64_tr_b16 v[158:159], %[w3] offset:38528\n\t"
            "v_mfma_f32_16x16x32_bf16 v[108:111], v[128:131], v[52:55], v[108:111]\n\t"
            "v_mfma_f32_16x16x32_bf16 v[92:95], v[132:135], v[48:51], v[92:95]\n\t"
            "v_mfma_f32_16x16x32_bf16 v[112:115], v[132:135], v[52:55], v[112:115]\n\t"
            "v_mfma_f32_16x16x32_bf16 v[96:99], v[136:139], v[48:51], v[96:99]\n\t"
            "v_mfma_f32_16x16x32_bf16 v[116:119], v[136:139], v[52:55], v[116:119]\n\t"
            "v_add_u32 %[w3], 51200, %[w3]\n\t"
            "s_waitcnt vmcnt(8) lgkmcnt(0)\n\t"
            "v_mfma_f32_16x16x32_bf16 v[80:83], v[140:143], v[56:59], v[80:83]\n\t"
            "ds_read_b64_tr_b16 v[120:121], %[w3] offset:0\n\t"
            "ds_read_b64_tr_b16 v[122:123], %[w3] offset:12800\n\t"
            "v_mfma_f32_16x16x32_bf16 v[100:103], v[140:143], v[60:63], v[100:103]\n\t"
            "ds_read_b64_tr_b16 v[124:125], %[w3] offset:32\n\t"
            "ds_read_b64_tr_b16 v[126:127], %[w3] offset:12832\n\t"
            "v_mfma_f32_16x16x32_bf16 v[84:87], v[144:147], v[56:59], v[84:87]\n\t"
            "ds_read_b64_tr_b16 v[128:129], %[w3] offset:64\n\t"
            "ds_read_b64_tr_b16 v[130:131], %[w3] offset:12864\n\t"
            "v_mfma_f32_16x16x32_bf16 v[104:107], v[144:147], v[60:63], v[104:107]\n\t"
            "ds_read_b64_tr_b16 v[132:133], %[w3] offset:96\n\t"
            "ds_read_b64_tr_b16 v[134:135], %[w3] offset:12896\n\t"
            "v_mfma_f32_16x16x32_bf16 v[88:91], v[148:151], v[56:59], v[88:91]\n\t"
            "ds_read_b64_tr_b16 v[136:137], %[w3] offset:128\n\t"
            "ds_read_b64_tr_b16 v[138:139], %[w3] offset:12928\n\t"
            "v_mfma_f32_16x16x32_bf16 v[108:111], v[148:151], v[60:63], v[108:111]\n\t"
            "v_mfma_f32_16x16x32_bf16 v[92:95], v[152:155], v[56:59], v[92:95]\n\t"
            "v_mfma_f32_16x16x32_bf16 v[112:115], v[152:155], v[60:63], v[112:115]\n\t"
            "v_mfma_f32_16x16x32_bf16 v[96:99], v[156:159], v[56:59], v[96:99]\n\t"
            "v_mfma_f32_16x16x32_bf16 v[116:119], v[156:159], v[60:63], v[116:119]\n\t"
            "s_waitcnt vmcnt(4) lgkmcnt(0)\n\t"
            "v_mfma_f32_16x16x32_bf16 v[80:83], v[120:123], v[64:67], v[80:83]\n\t"
            "ds_read_b64_tr_b16 v[140:141], %[w3] offset:25600\n\t"
            "ds_read_b64_tr_b16 v[142:143], %[w3] offset:38400\n\t"
            "v_mfma_f32_16x16x32_bf16 v[100:103], v[120:123], v[68:71], v[100:103]\n\t"
            "ds_read_b64_tr_b16 v[144:145], %[w3] offset:25632\n\t"
            "ds_read_b64_tr_b16 v[146:147], %[w3] offset:38432\n\t"
            "v_mfma_f32_16x16x32_bf16 v[84:87], v[124:127], v[64:67], v[84:87]\n\t"
            "ds_read_b64_tr_b16 v[148:149], %[w3] offset:25664\n\t"
            "ds_read_b64_tr_b16 v[150:151], %[w3] offset:38464\n\t"
            "v_mfma_f32_16x16x32_bf16 v[104:107], v[124:127], v[68:71], v[104:107]\n\t"
            "ds_read_b64_tr_b16 v[152:153], %[w3] offset:25696\n\t"
            "ds_read_b64_tr_b16 v[154:155], %[w3] offset:38496\n\t"
            "v_mfma_f32_16x16x32_bf16 v[88:91], v[128:131], v[64:67], v[88:91]\n\t"
            "ds_read_b64_tr_b16 v[156:157], %[w3] offset:25728\n\t"
            "ds_read_b64_tr_b16 v[158:159], %[w3] offset:38528\n\t"
            "v_mfma_f32_16x16x32_bf16 v[108:111], v[128:131], v[68:71], v[108:111]\n\t"
            "v_mfma_f32_16x16x32_bf16 v[92:95], v[132:135], v[64:67], v[92:95]\n\t"
            "v_mfma_f32_16x16x32_bf16 v[112:115], v[132:135], v[68:71], v[112:115]\n\t"
            "v_mfma_f32_16x16x32_bf16 v[96:99], v[136:139], v[64:67], v[96:99]\n\t"
            "v_mfma_f32_16x16x32_bf16 v[116:119], v[136:139], v[68:71], v[116:119]\n\t"
            "s_waitcnt vmcnt(0) lgkmcnt(0)\n\t"
            "v_mfma_f32_16x16x32_bf16 v[80:83], v[140:143], v[72:75], v[80:83]\n\t"
            "v_mfma_f32_16x16x32_bf16 v[100:103], v[140:143], v[76:79], v[100:103]\n\t"
            "v_mfma_f32_16x16x32_bf16 v[84:87], v[144:147], v[72:75], v[84:87]\n\t"
            "v_mfma_f32_16x16x32_bf16 v[104:107], v[144:147], v[76:79], v[104:107]\n\t"
            "v_mfma_f32_16x16x32_bf16 v[88:91], v[148:151], v[72:75], v[88:91]\n\t"
            "v_mfma_f32_16x16x32_bf16 v[108:111], v[148:151], v[76:79], v[108:111]\n\t"
            "v_mfma_f32_16x16x32_bf16 v[92:95], v[152:155], v[72:75], v[92:95]\n\t"
            "v_mfma_f32_16x16x32_bf16 v[112:115], v[152:155], v[76:79], v[112:115]\n\t"
            "v_mfma_f32_16x16x32_bf16 v[96:99], v[156:159], v[72:75], v[96:99]\n\t"
            "v_mfma_f32_16x16x32_bf16 v[116:119], v[156:159], v[76:79], v[116:119]\n\t"
            "s_nop 7\n\t"
                : "=&{v[80:111]}"(r0), "=&{v[112:115]}"(r1), "=&{v[116:119]}"(r2), [w1] "+v"(w1), [w2] "+v"(w2), [w3] "+v"(w3), [so] "=&s"(sof)
                : [oa] "v"(oa), [ob] "v"(ob), [srd] "s"(srd)
                : "memory", "scc", "v0", "v1", "v2", "v3", "v4", "v5", "v6", "v7", "v8", "v9", "v10", "v11", "v12", "v13", "v14", "v15", "v16", "v17", "v18", "v19", "v20", "v21", "v22", "v23", "v24", "v25", "v26", "v27", "v28", "v29", "v30", "v31", "v32", "v33", "v34", "v35", "v36", "v37", "v38", "v39", "v40", "v41", "v42", "v43", "v44", "v45", "v46", "v47", "v48", "v49", "v50", "v51", "v52", "v53", "v54", "v55", "v56", "v57", "v58", "v59", "v60", "v61", "v62", "v63", "v64", "v65", "v66", "v67", "v68", "v69", "v70", "v71", "v72", "v73", "v74", "v75", "v76", "v77", "v78", "v79", "v120", "v121", "v122", "v123", "v124", "v125", "v126", "v127", "v128", "v129", "v130", "v131", "v132", "v133", "v134", "v135", "v136", "v137", "v138", "v139", "v140", "v141", "v142", "v143", "v144", "v145", "v146", "v147", "v148", "v149", "v150", "v151", "v152", "v153", "v154", "v155", "v156", "v157", "v158", "v159");
            const __amdgpu_buffer_rsrc_t rout = __builtin_amdgcn_make_buffer_rsrc(out, 0, 2048 * 2560, 0x00020000);
#pragma unroll
            for (int f = 0; f < 5; f++) {
                const f32x4 v = f < 3 ? (f32x4){r0[20 + 4 * f], r0[21 + 4 * f], r0[22 + 4 * f], r0[23 + 4 * f]} : (f == 3 ? r1 : r2);
                const u32x4 w = swap16(pk2(r0[4 * f], r0[4 * f + 1]), pk2(r0[4 * f + 2], r0[4 * f + 3]), pk2(v[0], v[1]), pk2(v[2], v[3]));
                __builtin_amdgcn_raw_buffer_store_b128(w, rout, od + f * 32, 0, 0);
            }
        }
    } else {
        const __amdgpu_buffer_rsrc_t rq = __builtin_amdgcn_make_buffer_rsrc(ws, 0, 2 * WS_BR, 0x00020000);
        const int vd = vdim(r32);
        AttLane A;
        { const unsigned cha = (5 * hp + 3 * odd + h) * 32768, chb = (5 * hp + 2) * 32768 + odd * 8;
          const unsigned tq = r32 * 16, tk = ((r32 & 7) | ((r32 & 8) << 1) | ((r32 & 16) >> 1)) * 16;
          A.qa = cha + tq; A.qb0 = h ? OOB : chb + tq; A.qb1 = h ? chb + tq : OOB; A.ka = cha + tk; A.kb = chb + tk + h * 512; A.vo = r32 <= 20 ? vofs(head, vd, h) : OOB; }
        asm volatile("" : "+v"(A.qa), "+v"(A.qb0), "+v"(A.qb1), "+v"(A.ka), "+v"(A.kb), "+v"(A.vo));
        unsigned so = (head * 2 + h) * 32768 + (rb * 256 + r32) * 16, sb = (lane & 33) == 0 ? O_B + head * 16384 + (rb * 256 + lane) * 8 : OOB;
        unsigned fidx = lane < 32 ? F_B + ((ctab >> ((lane >> 3) * 4)) & 15) * 16 + 8 + (lane & 7) : F_A + ((ctab >> (((lane >> 1) & 7) * 4)) & 15) * 16 + 2 * rb + (lane & 1);
        unsigned kbA = h ? OOB : A.kb, av = r32 <= 20 ? vofs(head, r32, h) : OOB, pw = L_PL + rg * 12288 + (1 - kh) * 6144 + lane * 16, pr = L_PE + rg * 12288 + kh * 6144 + lane * 16, pa, sa;
        so += (rg * 4 + 2 * kh) * 512; sb += (rg * 4 + 2 * kh) * 256;
        u32x4 s16; unsigned s8;
        { const int reg = 4 * (r32 >> 3) + (r32 & 3); const bool fok = reg < 10;
          pa = L_WPHI + (10 * ((r32 >> 2) & 1) + reg) * 2; sa = fok ? 0x05040100u : 0x0c0c0c0cu;
          const unsigned one = (fok && ((r32 >> 2) & 1) == h) ? ((reg & 1) ? 0x3f800000u : 0x3f80u) : 0u;
#pragma unroll
          for (int i = 0; i < 4; i++) s16[i] = (reg >> 1) == i ? one : 0u;
          s8 = (reg >> 1) == 4 ? one : 0u; }
        asm volatile("" : "+v"(fidx), "+v"(kbA), "+v"(av), "+v"(pw), "+v"(pr), "+v"(pa), "+v"(sa), "+v"(s16), "+v"(s8), "+v"(so), "+v"(sb));
        if (has_tile) {
            const int wm = wid >> 1, wn = wid & 1, g = lane >> 4, q = (lane & 15) >> 2, p = lane & 3;
            Comp C;
            { const int r = wm * 64 + (lane & 15);
#pragma unroll
              for (int ks = 0; ks < 2; ks++) C.xa[ks] = r * 128 + (((4 * ks + g) ^ ((r >> 1) & 7)) << 4);
#pragma unroll
              for (int f = 0; f < 4; f++) C.wa[f] = (8 * g + q) * 256 + (((wn * 8 + 2 * f + (p >> 1)) ^ ((q | ((g & 1) << 2)) << 1)) << 4) + (p & 1) * 8; }
            const u32x4 srd = mksrd(ws + WS_BR, WS_BR);
            f32x32 a[2];
            if (nt < 10) {
#pragma unroll
                for (int f = 0; f < 4; f++) C.so[f] = (nt * 16 + wn * 8 + 2 * f + h) * 32768 + (mt * 128 + wm * 64 + r32) * 16;
                gemm_wx(C, srd, nt < 5 ? 0.32259641822f : 1.0f, DRAIN, a[0], a[1]);
#pragma unroll
                for (int tp = 0; tp < 2; tp++)
#pragma unroll
                    for (int f = 0; f < 4; f++) { const f32x32& v = a[tp];
                        __builtin_amdgcn_raw_buffer_store_b128(swap16(pk2(v[4 * f], v[4 * f + 1]), pk2(v[4 * f + 2], v[4 * f + 3]), pk2(v[16 + 4 * f], v[17 + 4 * f]), pk2(v[18 + 4 * f], v[19 + 4 * f])), rq, C.so[f] + 512 * tp, 0, SC1); }
            } else {
                const unsigned vb = 2 * SEC + (nt - 10) * V_C + (mt * 4 + wm * 2) * V_T;
                C.so[0] = vb + ((g & 1) * 2 + (g >> 1)) * V_G + (wn * 64 + (lane & 15)) * 16; C.so[1] = C.so[0] + V_T; C.so[2] = C.so[3] = 0;
                C.on = wn * WS_BR + vb + (lane >> 3) * V_G + 2048 + (lane & 7) * 16;
                gemm_xw(C, srd, mksrd(ws, 2 * WS_BR), 1.0f, DRAIN, a[0], a[1]);
#pragma unroll
                for (int tp = 0; tp < 2; tp++)
#pragma unroll
                    for (int f = 0; f < 4; f++) { const f32x32& v = a[tp];
                        __builtin_amdgcn_raw_buffer_store_b128((u32x4){pk2(v[4 * f], v[4 * f + 1]), pk2(v[4 * f + 2], v[4 * f + 3]), pk2(v[16 + 4 * f], v[17 + 4 * f]), pk2(v[18 + 4 * f], v[19 + 4 * f])}, rq, C.so[tp] + 256 * f, 0, SC1); }
            }
        } else asm volatile("s_waitcnt vmcnt(0)\n\ts_barrier" ::: "memory");
        __builtin_amdgcn_s_setprio(3);
        const u64 tag = call_tag(ctl);
        u32x4 wa; u32x2 wb;
        { const LAS u16* p0 = (const LAS u16*)(pa + (4 * odd + 8 * h) * 40); const LAS u16* p1 = (const LAS u16*)(pa + (odd ? 0 : 16) * 40); const unsigned sh = h ? 0x0c0c0c0cu : sa;
#pragma unroll
          for (int i = 0; i < 4; i++) wa[i] = __builtin_amdgcn_perm(p0[(2 * i + 1) * 20], p0[2 * i * 20], sa);
#pragma unroll
          for (int i = 0; i < 2; i++) wb[i] = __builtin_amdgcn_perm(p1[(2 * i + 1) * 20], p1[2 * i * 20], sh); }
        asm volatile("" : "+v"(wa), "+v"(wb));
        drain_and_flag(ctl + C_ADRAIN, flagp(ws, F_A + nt * 16 + mt), tag, has_tile);
        if (wid == 0) {
            wait_lds<1>(ctl + C_ADRAIN, 4u);
            wait_flags(ws, fidx, tag);
            wait_lds<1>(ctl + C_BOK, 1u);
            __hip_atomic_store(ctl + C_AOK, 1u, RLX_WG);
        } else wait_lds<2>(ctl + C_AOK, 1u);
        u32x4 ka[2], aa[2], v0[2], v1[2]; u32x2 kb[2], ab[2];
#pragma unroll
        for (int i = 0; i < 2; i++) {
            const unsigned T = rb * 8 + wid * 2 + i, sv = 2 * SEC + T * V_T;
            ka[i] = ld16(rq, A.ka, SEC + T * 512); kb[i] = ld8(rq, kbA, SEC + T * 512); v0[i] = ld16(rq, av, sv); v1[i] = ld16(rq, av + V_G, sv);
            aa[i] = ld16(rq, A.qa, T * 512); ab[i] = ld8(rq, A.qb0, T * 512);
        }
        asm volatile("" : "+v"(ka[0]), "+v"(kb[0]), "+v"(v0[0]), "+v"(v1[0]), "+v"(aa[0]), "+v"(ab[0]), "+v"(ka[1]), "+v"(kb[1]), "+v"(v0[1]), "+v"(v1[1]), "+v"(aa[1]), "+v"(ab[1]));
        const f32x16 z16 = {};
        f32x16 kv[2] = {};
#pragma unroll
        for (int i = 0; i < 2; i++) {
            unsigned ph[10];
            feature_map(MFMA8(wb, kb[i], MFMA16(wa, ka[i], z16)), ph);
#pragma unroll
            for (int sg = 0; sg < 2; sg++) {
                const f32x16 tr = MFMA8(((u32x2){ph[5 * sg + 4], 0u}), ((u32x2){s8, 0u}), MFMA16(((u32x4){ph[5 * sg], ph[5 * sg + 1], ph[5 * sg + 2], ph[5 * sg + 3]}), s16, z16));
                kv[sg] = MFMA16(pk8(tr, 8), v1[i], MFMA16(pk8(tr, 0), v0[i], kv[sg]));
            }
        }
        f32x4 rec[5] = {{kv[0][0], kv[0][1], kv[0][2], kv[0][3]}, {kv[0][4], kv[0][5], kv[0][6], kv[0][7]}, {kv[0][8], kv[0][9], kv[1][0], kv[1][1]}, {kv[1][2], kv[1][3], kv[1][4], kv[1][5]}, {kv[1][6], kv[1][7], kv[1][8], kv[1][9]}};
        if (wid > 0) {
            if (r32 <= 20) {
#pragma unroll
                for (int i = 0; i < 5; i++) *(LAS f32x4*)(lds + L_KVW + (wid - 1) * 3360 + (r32 * 2 + h) * 80 + i * 16) = rec[i];
            }
            lds_arrive(ctl + C_KVW);
        } else {
            wait_lds<1>(ctl + C_KVW, 3u);
            const __amdgpu_buffer_rsrc_t rs = __builtin_amdgcn_make_buffer_rsrc(ws + WS_KVP, 0, 256 * 4096, 0x00020000);
            if (r32 <= 20) {
#pragma unroll
                for (int i = 0; i < 5; i++) {
#pragma unroll
                    for (int w = 0; w < 3; w++) rec[i] += *(LAS f32x4*)(lds + L_KVW + w * 3360 + (r32 * 2 + h) * 80 + i * 16);
                    __builtin_amdgcn_raw_buffer_store_b128(__builtin_bit_cast(u32x4, rec[i]), rs, (head * 8 + rb) * 4096 + (r32 * 2 + h) * 80 + i * 16, 0, SC1);
                }
            }
        }
#pragma unroll
        for (int i = 0; i < 2; i++) {
            unsigned ph[10];
            feature_map(MFMA8(wb, ab[i], MFMA16(wa, aa[i], z16)), ph);
            LAS char* pp = (LAS char*)lds + L_PHQ + (wid * 2 + i) * 2560;
            *(LAS u32x4*)(pp + lane * 16) = (u32x4){ph[0], ph[1], ph[2], ph[3]}; *(LAS u32x4*)(pp + 1024 + lane * 16) = (u32x4){ph[4], ph[5], ph[6], ph[7]}; *(LAS u32x2*)(pp + 2048 + lane * 8) = (u32x2){ph[8], ph[9]};
        }
        lds_arrive(ctl + C_PHQ);
        if (wid == 0) { asm volatile("s_waitcnt vmcnt(0)" ::: "memory"); if (lane == 0) __hip_atomic_store(flagp(ws, F_KV + head * 8 + rb), tag, RLX_AGENT); }
        f32x16 o[2];
        const int tile0 = rb * 8 + rg * 4 + 2 * kh;
        __builtin_amdgcn_s_setprio(0);
        attend_finish(ws, SEC + (2 + kh) * 16 * 512, 2 * SEC + (2 + kh) * 16 * V_T, (rb * 8 + rg * 4 + 2 - 2 * kh) * 512, tile0 * 512, A, pw, pr, L_CTL + (C_PE + rg) * 4, o[0], o[1]);
        __builtin_amdgcn_s_setprio(3);
        float l[2]; f32x2 nf = {};
#pragma unroll
        for (int t = 0; t < 2; t++) { l[t] = upper(o[t][8]);
#pragma unroll
            for (int k = 0; k < 6; k++) nf = __builtin_elementwise_fma((f32x2){o[t][2 * k], o[t][2 * k + 1]}, (f32x2){0.f, 0.f}, nf); }
        if (__builtin_expect(__any(!((fminf(l[0], l[1]) >= L_MIN) & (fmaxf(l[0], l[1]) <= 1.0e36f) & (nf[0] + nf[1] == 0.f))), 0)) {
#pragma unroll 1
            for (int t = 0; t < 2; t++) {
                const u32x4 fa = ld16(rq, A.qa, WS_BR + (tile0 + t) * 512); const u32x2 fb = ld8(rq, A.qb0, WS_BR + (tile0 + t) * 512);
                const unsigned vp = vofs(head, vd, h);
                f32x16 oo = {}; float mr = -INFINITY;
#pragma unroll 1
                for (int T = 0; T < 64; T++) {
                    const u32x4 xa = ld16(rq, A.ka, WS_BR + SEC + T * 512); const u32x2 xb = ld8(rq, kbA, WS_BR + SEC + T * 512);
                    const u32x4 y0 = ld16(rq, vp, WS_BR + 2 * SEC + T * V_T), y1 = ld16(rq, vp + V_G, WS_BR + 2 * SEC + T * V_T);
                    f32x16 s = MFMA8(xb, fb, MFMA16(xa, fa, z16));
                    float mx = mr;
#pragma unroll
                    for (int k = 0; k < 16; k++) mx = fmaxf(mx, s[k]);
                    mx = swap_max(mx);
                    const float al = __builtin_amdgcn_exp2f(mr - mx); mr = mx;
#pragma unroll
                    for (int k = 0; k < 16; k++) { s[k] = __builtin_amdgcn_exp2f(s[k] - mx); oo[k] *= al; }
                    oo = MFMA16(y1, pk8(s, 8), MFMA16(y0, pk8(s, 0), oo));
                }
                if (t) { o[1] = oo; l[1] = oo[12]; } else { o[0] = oo; l[0] = oo[12]; }
            }
        }
        const __amdgpu_buffer_rsrc_t ro = __builtin_amdgcn_make_buffer_rsrc(ws + WS_O + SEC, 0, SEC, 0x00020000);
#pragma unroll
        for (int t = 0; t < 2; t++) store_o(ro, so + t * 512, sb + t * 256, o[t], __builtin_amdgcn_rcpf(l[t]));
        drain_and_flag(ctl + C_OB, flagp(ws, F_O + 256 + rb * 32 + head), tag, true);
        if (blockIdx.x == 0 && wid == 0) {
#pragma unroll 1
            for (int q = 0; q < 4; q++) wait_flags(ws, F_KV + q * 64 + lane, tag);
            if (lane == 0) __hip_atomic_store(flagp(ws, 0), tag, RLX_AGENT);
        }
    }
}

extern "C" void kernel_launch(void* const* d_in, const int* in_sizes, int n_in, void* d_out, int out_size, void* d_ws, size_t ws_size, hipStream_t stream) {
    (void)in_sizes; (void)n_in; (void)out_size; (void)ws_size;
    (void)hipFuncSetAttribute((const void*)k_main, hipFuncAttributeMaxDynamicSharedMemorySize, L_MAIN);
    k_main<<<256, 512, L_MAIN, stream>>>((const char*)d_in[0], (const char*)d_in[1], (const u16*)d_in[2], (const char*)d_in[3], (const char*)d_in[4], (const char*)d_in[5], (const char*)d_in[6], (char*)d_ws, (char*)d_out);
}
```
